```python
import torch

def _softmax(x, axis=-1):
    return torch.softmax(x, dim=axis)
def _gelu(x):
    return torch.nn.functional.gelu(x, approximate="tanh")
def _silu(x):
    return torch.nn.functional.silu(x)
def _sigmoid(x):
    return torch.sigmoid(x)
def _layernorm(x, axis=-1, eps=1e-5):
    m = x.mean(dim=axis, keepdim=True)
    v = x.var(dim=axis, keepdim=True, unbiased=False)
    return (x - m) / torch.sqrt(v + eps)
def _scatter_add(tgt, idx, src, axis=0):
    return tgt.clone().index_add_(axis, idx, src)
def _segment_sum(src, seg, n):
    out = torch.zeros((n,) + src.shape[1:], dtype=src.dtype)
    return out.index_add_(0, seg, src)
def _pad(x, pw):
    flat = tuple(v for pair in reversed(pw) for v in pair)
    return torch.nn.functional.pad(x, flat)

kernel_name = 'recurrent_scan__spectral_fnet__geglu_ffn__prompt'

def setup_inputs():

    _w = (lambda a: (lambda t: t.to(torch.bfloat16) if t.is_floating_point() else t)(torch.as_tensor(a)))
    def _A_setup():
        B, S, D = 1, 2048, 448
        ns = torch.arange(S)[:, None] * torch.arange(S)[None, :]
        nd = torch.arange(D)[:, None] * torch.arange(D)[None, :]
        Ws_re = torch.cos(-2.0 * torch.pi * ns / S).to(float)
        Ws_im = torch.sin(-2.0 * torch.pi * ns / S).to(float)
        Wd_re = torch.cos(-2.0 * torch.pi * nd / D).to(float)
        Wd_im = torch.sin(-2.0 * torch.pi * nd / D).to(float)
        return {
            "x": _w((lambda *s: torch.randn(*s, dtype=torch.bfloat16) * 0.05)(B, S, D)),
            "dft_seq_re": _w(Ws_re),
            "dft_seq_im": _w(Ws_im),
            "dft_hid_re": _w(Wd_re),
            "dft_hid_im": _w(Wd_im),
        }
    _A_in = _A_setup()

    def _B_setup():
        B, S, D, F = 1, 2048, 448, 1792
        return {
            "x": _w((lambda *s: torch.randn(*s, dtype=torch.bfloat16) * 0.05)(B, S, D)),
            "w_gate": _w((lambda *s: torch.randn(*s, dtype=torch.bfloat16) * 0.05)(D, F)),
            "w_up": _w((lambda *s: torch.randn(*s, dtype=torch.bfloat16) * 0.05)(D, F)),
            "w_down": _w((lambda *s: torch.randn(*s, dtype=torch.bfloat16) * 0.05)(F, D)),
        }
    _B_in = _B_setup()

    return {
        "A_x": _A_in["x"],
        "A_dft_seq_re": _A_in["dft_seq_re"],
        "A_dft_seq_im": _A_in["dft_seq_im"],
        "A_dft_hid_re": _A_in["dft_hid_re"],
        "A_dft_hid_im": _A_in["dft_hid_im"],
        "B_x": _B_in["x"],
        "B_w_gate": _B_in["w_gate"],
        "B_w_up": _B_in["w_up"],
        "B_w_down": _B_in["w_down"],
    }

def reference(A_x, A_dft_seq_re, A_dft_seq_im, A_dft_hid_re, A_dft_hid_im, B_x, B_w_gate, B_w_up, B_w_down):
    def _A_ref(x, dft_seq_re, dft_seq_im, dft_hid_re, dft_hid_im):
        a = torch.einsum("bsd,ed->bse", x, dft_hid_re)
        b = torch.einsum("bsd,ed->bse", x, dft_hid_im)
        y = torch.einsum("ts,bse->bte", dft_seq_re, a) - torch.einsum("ts,bse->bte", dft_seq_im, b)
        return y

    def _B_ref(x, w_gate, w_up, w_down):
        return (_gelu(x @ w_gate) * (x @ w_up)) @ w_down

    x = A_x
    xn = _layernorm(x, axis=-1)
    s = _A_ref(xn, A_dft_seq_re, A_dft_seq_im, A_dft_hid_re, A_dft_hid_im)
    g = _silu(_B_ref(xn, B_w_gate, B_w_up, B_w_down))
    return x + s * g
```

```cpp
#include <hip/hip_runtime.h>
#include <hip/hip_bf16.h>
#include <stdint.h>

typedef unsigned short u16;
#define MIMIC 0
__device__ __forceinline__ float bf2f(u16 b){ return __uint_as_float(((unsigned)b)<<16); }
__device__ __forceinline__ u16 f2bf(float f){ unsigned u=__float_as_uint(f); u += 0x7FFFu + ((u>>16)&1u); return (u16)(u>>16); }
__device__ __forceinline__ float rb(float f){ return bf2f(f2bf(f)); }

constexpr int S=2048, D=448, F=1792;

__global__ void ln_k(const u16* x, u16* xn){
  int row=blockIdx.x; int lane=threadIdx.x;
  float v[7]; float s=0;
  for(int i=0;i<7;i++){ v[i]=bf2f(x[row*D+lane+64*i]); s+=v[i]; }
  for(int o=32;o>=1;o>>=1) s+=__shfl_xor(s,o);
  float m=s/D; float q=0;
  for(int i=0;i<7;i++){ float d=v[i]-m; q+=d*d; }
  for(int o=32;o>=1;o>>=1) q+=__shfl_xor(q,o);
  float var=q/D;
  float r=1.0f/sqrtf(var+1e-5f);
  for(int i=0;i<7;i++) xn[row*D+lane+64*i]=f2bf((v[i]-m)*r);
}

__global__ void gemm_k(const u16* A,int lda,const u16* B,int sbk,int sbn,float* C,int ldc,int M,int N,int K){
  __shared__ float As[16][17], Bs[16][17];
  int tx=threadIdx.x, ty=threadIdx.y;
  int m=blockIdx.y*16+ty, n=blockIdx.x*16+tx;
  float acc=0;
  for(int k0=0;k0<K;k0+=16){
    As[ty][tx]=bf2f(A[(size_t)(blockIdx.y*16+ty)*lda+k0+tx]);
    Bs[ty][tx]=bf2f(B[(size_t)(k0+ty)*sbk+(size_t)(blockIdx.x*16+tx)*sbn]);
    __syncthreads();
    for(int k=0;k<16;k++) acc+=As[ty][k]*Bs[k][tx];
    __syncthreads();
  }
  C[(size_t)m*ldc+n]=acc;
}
__global__ void cvt_k(const float* in,u16* out,int n){ int i=blockIdx.x*256+threadIdx.x; if(i<n) out[i]=f2bf(in[i]); }
__device__ __forceinline__ float gelu_t(float x){ float u=0.7978845608028654f*(x+0.044715f*x*x*x); return 0.5f*x*(1.0f+tanhf(u)); }
__global__ void h_k(const float* g,const float* u,u16* h,int n){ int i=blockIdx.x*256+threadIdx.x; if(i<n){
  h[i]=f2bf(gelu_t(g[i])*u[i]);
}}
__global__ void fin_k(const u16* x,const float* y1,const float* y2,const float* f,u16* out,int n){ int i=blockIdx.x*256+threadIdx.x; if(i<n){
  float s=y1[i]-y2[i]; float ff=f[i]; float g=ff/(1.0f+__expf(-ff));
  out[i]=f2bf(bf2f(x[i])+s*g);
}}

extern "C" void kernel_launch(void* const* d_in, const int* in_sizes, int n_in, void* d_out, int out_size, void* d_ws, size_t ws_size, hipStream_t stream){
  const u16* x=(const u16*)d_in[0]; const u16* Cr=(const u16*)d_in[1]; const u16* Ci=(const u16*)d_in[2];
  const u16* Wr=(const u16*)d_in[3]; const u16* Wi=(const u16*)d_in[4];
  const u16* Wg=(const u16*)d_in[6]; const u16* Wu=(const u16*)d_in[7]; const u16* Wd=(const u16*)d_in[8];
  char* ws=(char*)d_ws; size_t o=0;
  auto al=[&](size_t b){ void* p=ws+o; o+=(b+255)/256*256; return p; };
  u16* xn=(u16*)al(S*D*2);
  float* t0=(float*)al((size_t)S*F*4); float* t1=(float*)al((size_t)S*F*4);
  u16* a=(u16*)al(S*D*2); u16* b=(u16*)al(S*D*2); u16* h=(u16*)al((size_t)S*F*2);
  float* y1=(float*)al(S*D*4); float* y2=(float*)al(S*D*4); float* f=(float*)al(S*D*4);
  ln_k<<<S,64,0,stream>>>(x,xn);
  dim3 tb(16,16);
  gemm_k<<<dim3(D/16,S/16),tb,0,stream>>>(xn,D,Wr,1,D,t0,D,S,D,D);
  cvt_k<<<(S*D+255)/256,256,0,stream>>>(t0,a,S*D);
  gemm_k<<<dim3(D/16,S/16),tb,0,stream>>>(xn,D,Wi,1,D,t0,D,S,D,D);
  cvt_k<<<(S*D+255)/256,256,0,stream>>>(t0,b,S*D);
  gemm_k<<<dim3(D/16,S/16),tb,0,stream>>>(Cr,S,a,D,1,y1,D,S,D,S);
  gemm_k<<<dim3(D/16,S/16),tb,0,stream>>>(Ci,S,b,D,1,y2,D,S,D,S);
  gemm_k<<<dim3(F/16,S/16),tb,0,stream>>>(xn,D,Wg,F,1,t0,F,S,F,D);
  gemm_k<<<dim3(F/16,S/16),tb,0,stream>>>(xn,D,Wu,F,1,t1,F,S,F,D);
  h_k<<<(S*F+255)/256,256,0,stream>>>(t0,t1,h,S*F);
  gemm_k<<<dim3(D/16,S/16),tb,0,stream>>>(h,F,Wd,D,1,f,D,S,D,F);
  fin_k<<<(S*D+255)/256,256,0,stream>>>(x,y1,y2,f,(u16*)d_out,S*D);
}
```

```cpp
#include <hip/hip_runtime.h>
#include <hip/hip_bf16.h>
#include <stdint.h>

typedef unsigned short u16;
#define MIMIC 1
__device__ __forceinline__ float bf2f(u16 b){ return __uint_as_float(((unsigned)b)<<16); }
__device__ __forceinline__ u16 f2bf(float f){ unsigned u=__float_as_uint(f); u += 0x7FFFu + ((u>>16)&1u); return (u16)(u>>16); }
__device__ __forceinline__ float rb(float f){ return bf2f(f2bf(f)); }

constexpr int S=2048, D=448, F=1792;

__global__ void ln_k(const u16* x, u16* xn){
  int row=blockIdx.x; int lane=threadIdx.x;
  float v[7]; float s=0;
  for(int i=0;i<7;i++){ v[i]=bf2f(x[row*D+lane+64*i]); s+=v[i]; }
  for(int o=32;o>=1;o>>=1) s+=__shfl_xor(s,o);
  float m=s/D; float q=0;
  for(int i=0;i<7;i++){ float d=v[i]-m; q+=d*d; }
  for(int o=32;o>=1;o>>=1) q+=__shfl_xor(q,o);
  float var=q/D;
  float mb=rb(m), vb=rb(var);
  float den=rb(sqrtf(rb(vb+1e-5f)));
  for(int i=0;i<7;i++) xn[row*D+lane+64*i]=f2bf(rb(v[i]-mb)/den);
}

__global__ void gemm_k(const u16* A,int lda,const u16* B,int sbk,int sbn,float* C,int ldc,int M,int N,int K){
  __shared__ float As[16][17], Bs[16][17];
  int tx=threadIdx.x, ty=threadIdx.y;
  int m=blockIdx.y*16+ty, n=blockIdx.x*16+tx;
  float acc=0;
  for(int k0=0;k0<K;k0+=16){
    As[ty][tx]=bf2f(A[(size_t)(blockIdx.y*16+ty)*lda+k0+tx]);
    Bs[ty][tx]=bf2f(B[(size_t)(k0+ty)*sbk+(size_t)(blockIdx.x*16+tx)*sbn]);
    __syncthreads();
    for(int k=0;k<16;k++) acc+=As[ty][k]*Bs[k][tx];
    __syncthreads();
  }
  C[(size_t)m*ldc+n]=acc;
}
__global__ void cvt_k(const float* in,u16* out,int n){ int i=blockIdx.x*256+threadIdx.x; if(i<n) out[i]=f2bf(in[i]); }
__device__ __forceinline__ float gelu_t(float x){ float u=0.7978845608028654f*(x+0.044715f*x*x*x); return 0.5f*x*(1.0f+tanhf(u)); }
__global__ void h_k(const float* g,const float* u,u16* h,int n){ int i=blockIdx.x*256+threadIdx.x; if(i<n){
  h[i]=f2bf(rb(gelu_t(rb(g[i])))*rb(u[i]));
}}
__global__ void fin_k(const u16* x,const float* y1,const float* y2,const float* f,u16* out,int n){ int i=blockIdx.x*256+threadIdx.x; if(i<n){
  float s=rb(rb(y1[i])-rb(y2[i])); float ff=rb(f[i]); float g=rb(ff/(1.0f+__expf(-ff)));
  out[i]=f2bf(bf2f(x[i])+rb(s*g));
}}

extern "C" void kernel_launch(void* const* d_in, const int* in_sizes, int n_in, void* d_out, int out_size, void* d_ws, size_t ws_size, hipStream_t stream){
  const u16* x=(const u16*)d_in[0]; const u16* Cr=(const u16*)d_in[1]; const u16* Ci=(const u16*)d_in[2];
  const u16* Wr=(const u16*)d_in[3]; const u16* Wi=(const u16*)d_in[4];
  const u16* Wg=(const u16*)d_in[6]; const u16* Wu=(const u16*)d_in[7]; const u16* Wd=(const u16*)d_in[8];
  char* ws=(char*)d_ws; size_t o=0;
  auto al=[&](size_t b){ void* p=ws+o; o+=(b+255)/256*256; return p; };
  u16* xn=(u16*)al(S*D*2);
  float* t0=(float*)al((size_t)S*F*4); float* t1=(float*)al((size_t)S*F*4);
  u16* a=(u16*)al(S*D*2); u16* b=(u16*)al(S*D*2); u16* h=(u16*)al((size_t)S*F*2);
  float* y1=(float*)al(S*D*4); float* y2=(float*)al(S*D*4); float* f=(float*)al(S*D*4);
  ln_k<<<S,64,0,stream>>>(x,xn);
  dim3 tb(16,16);
  gemm_k<<<dim3(D/16,S/16),tb,0,stream>>>(xn,D,Wr,1,D,t0,D,S,D,D);
  cvt_k<<<(S*D+255)/256,256,0,stream>>>(t0,a,S*D);
  gemm_k<<<dim3(D/16,S/16),tb,0,stream>>>(xn,D,Wi,1,D,t0,D,S,D,D);
  cvt_k<<<(S*D+255)/256,256,0,stream>>>(t0,b,S*D);
  gemm_k<<<dim3(D/16,S/16),tb,0,stream>>>(Cr,S,a,D,1,y1,D,S,D,S);
  gemm_k<<<dim3(D/16,S/16),tb,0,stream>>>(Ci,S,b,D,1,y2,D,S,D,S);
  gemm_k<<<dim3(F/16,S/16),tb,0,stream>>>(xn,D,Wg,F,1,t0,F,S,F,D);
  gemm_k<<<dim3(F/16,S/16),tb,0,stream>>>(xn,D,Wu,F,1,t1,F,S,F,D);
  h_k<<<(S*F+255)/256,256,0,stream>>>(t0,t1,h,S*F);
  gemm_k<<<dim3(D/16,S/16),tb,0,stream>>>(h,F,Wd,D,1,f,D,S,D,F);
  fin_k<<<(S*D+255)/256,256,0,stream>>>(x,y1,y2,f,(u16*)d_out,S*D);
}
```

```cpp
#include <hip/hip_runtime.h>
#include <hip/hip_bf16.h>
#include <stdint.h>

typedef unsigned short u16;
#define MIMIC 1
__device__ __forceinline__ float bf2f(u16 b){ return __uint_as_float(((unsigned)b)<<16); }
__device__ __forceinline__ u16 f2bf(float f){ unsigned u=__float_as_uint(f); u += 0x7FFFu + ((u>>16)&1u); return (u16)(u>>16); }
__device__ __forceinline__ float rb(float f){ return bf2f(f2bf(f)); }

constexpr int S=2048, D=448, F=1792;

__global__ void ln_k(const u16* x, u16* xn){
  int row=blockIdx.x; int lane=threadIdx.x;
  float v[7]; float s=0;
  for(int i=0;i<7;i++){ v[i]=bf2f(x[row*D+lane+64*i]); s+=v[i]; }
  for(int o=32;o>=1;o>>=1) s+=__shfl_xor(s,o);
  float m=s/D; float q=0;
  for(int i=0;i<7;i++){ float d=v[i]-m; q+=d*d; }
  for(int o=32;o>=1;o>>=1) q+=__shfl_xor(q,o);
  float var=q/D;
  float mb=rb(m), vb=rb(var);
  float den=rb(sqrtf(rb(vb+1e-5f)));
  for(int i=0;i<7;i++) xn[row*D+lane+64*i]=f2bf(rb(v[i]-mb)/den);
}

__global__ void gemm_k(const u16* A,int lda,const u16* B,int sbk,int sbn,float* C,int ldc,int M,int N,int K){
  __shared__ float As[16][17], Bs[16][17];
  int tx=threadIdx.x, ty=threadIdx.y;
  int m=blockIdx.y*16+ty, n=blockIdx.x*16+tx;
  float acc=0;
  for(int k0=0;k0<K;k0+=16){
    As[ty][tx]=bf2f(A[(size_t)(blockIdx.y*16+ty)*lda+k0+tx]);
    Bs[ty][tx]=bf2f(B[(size_t)(k0+ty)*sbk+(size_t)(blockIdx.x*16+tx)*sbn]);
    __syncthreads();
    for(int k=0;k<16;k++) acc+=As[ty][k]*Bs[k][tx];
    __syncthreads();
  }
  C[(size_t)m*ldc+n]=acc;
}
__global__ void cvt_k(const float* in,u16* out,int n){ int i=blockIdx.x*256+threadIdx.x; if(i<n) out[i]=f2bf(in[i]); }
__device__ __forceinline__ float gelu_t(float x){ float u=0.7978845608028654f*(x+0.044715f*x*x*x); return 0.5f*x*(1.0f+tanhf(u)); }
__global__ void h_k(const float* g,const float* u,u16* h,int n){ int i=blockIdx.x*256+threadIdx.x; if(i<n){
  h[i]=f2bf(rb(gelu_t(rb(g[i])))*rb(u[i]));
}}
__global__ void fin_k(const u16* x,const float* y1,const float* y2,const float* f,u16* out,int n){ int i=blockIdx.x*256+threadIdx.x; if(i<n){
  float s=rb(rb(y1[i])-rb(y2[i])); float ff=rb(f[i]); float g=rb(ff/(1.0f+__expf(-ff)));
  out[i]=f2bf(bf2f(x[i])+rb(s*g));
}}

#define LAS __attribute__((address_space(3)))
template<int AUX>
__global__ __launch_bounds__(512) void probe_k(const char* A, const char* B, size_t bytes_per_wg, unsigned long long* st){
  extern __shared__ __attribute__((aligned(16))) char shm[];
  const int tid=threadIdx.x, wid=__builtin_amdgcn_readfirstlane(tid>>6), lane=tid&63;
  int bid=blockIdx.x;
  unsigned long long t0=__builtin_amdgcn_s_memrealtime();
  const char* P = (bid&1)? B : A;
  const char* base = P + (size_t)(bid>>1)*bytes_per_wg + wid*4*1024 + lane*16;
  constexpr int SLOT=8*4*1024;
  int nsteps=bytes_per_wg/SLOT;
  int slot=0;
  for(int s=0;s<nsteps;s++){
    #pragma unroll
    for(int i=0;i<4;i++)
      __builtin_amdgcn_global_load_lds((const unsigned*)(base+(size_t)s*SLOT+i*1024),(LAS unsigned*)(shm+slot*SLOT+(wid*4+i)*1024),16,0,AUX);
    slot++; if(slot==4) slot=0;
    asm volatile("s_waitcnt vmcnt(12)":::"memory");
    __builtin_amdgcn_s_barrier();
  }
  asm volatile("s_waitcnt vmcnt(0)":::"memory");
  __builtin_amdgcn_s_barrier();
  unsigned long long t1=__builtin_amdgcn_s_memrealtime();
  if(tid==0){ st[bid*2+0]=t0; st[bid*2+1]=t1; }
}
template<int AUX>
__global__ __launch_bounds__(512) void probew_k(char* A,size_t bytes_per_wg,unsigned long long* st){
  int tid=threadIdx.x; int bid=blockIdx.x;
  unsigned long long t0=__builtin_amdgcn_s_memrealtime();
  auto rs=__builtin_amdgcn_make_buffer_rsrc(A+(size_t)bid*bytes_per_wg,0,(int)bytes_per_wg,0x00020000);
  typedef unsigned u32x4 __attribute__((ext_vector_type(4)));
  u32x4 v={1u,2u,3u,(unsigned)tid};
  for(size_t o=tid*16;o<bytes_per_wg;o+=512*16) __builtin_amdgcn_raw_buffer_store_b128(v,rs,(int)o,0,AUX);
  asm volatile("s_waitcnt vmcnt(0)":::"memory");
  __syncthreads();
  unsigned long long t1=__builtin_amdgcn_s_memrealtime();
  if(tid==0){ st[bid*2+0]=t0; st[bid*2+1]=t1; }
}
__global__ void amp_k(const unsigned long long* st,int n,int amp){
  unsigned long long mn=~0ull,mx=0;
  for(int b=0;b<n;b++){ unsigned long long a=st[b*2],c=st[b*2+1]; mn=a<mn?a:mn; mx=c>mx?c:mx; }
  unsigned long long T=(mx-mn); if(T>100000) T=100000;
  unsigned long long t0=__builtin_amdgcn_s_memrealtime();
  while(__builtin_amdgcn_s_memrealtime()-t0 < T*amp) __builtin_amdgcn_s_sleep(4);
}

extern "C" void kernel_launch(void* const* d_in, const int* in_sizes, int n_in, void* d_out, int out_size, void* d_ws, size_t ws_size, hipStream_t stream){
  const u16* x=(const u16*)d_in[0]; const u16* Cr=(const u16*)d_in[1]; const u16* Ci=(const u16*)d_in[2];
  const u16* Wr=(const u16*)d_in[3]; const u16* Wi=(const u16*)d_in[4];
  const u16* Wg=(const u16*)d_in[6]; const u16* Wu=(const u16*)d_in[7]; const u16* Wd=(const u16*)d_in[8];
  char* ws=(char*)d_ws; size_t o=0;
  auto al=[&](size_t b){ void* p=ws+o; o+=(b+255)/256*256; return p; };
  u16* xn=(u16*)al(S*D*2);
  float* t0=(float*)al((size_t)S*F*4); float* t1=(float*)al((size_t)S*F*4);
  u16* a=(u16*)al(S*D*2); u16* b=(u16*)al(S*D*2); u16* h=(u16*)al((size_t)S*F*2);
  float* y1=(float*)al(S*D*4); float* y2=(float*)al(S*D*4); float* f=(float*)al(S*D*4);
  unsigned long long* st=(unsigned long long*)(ws+(90u<<20));
  ln_k<<<S,64,0,stream>>>(x,xn);
  dim3 tb(16,16);
  gemm_k<<<dim3(D/16,S/16),tb,0,stream>>>(xn,D,Wr,1,D,t0,D,S,D,D);
  cvt_k<<<(S*D+255)/256,256,0,stream>>>(t0,a,S*D);
  gemm_k<<<dim3(D/16,S/16),tb,0,stream>>>(xn,D,Wi,1,D,t0,D,S,D,D);
  cvt_k<<<(S*D+255)/256,256,0,stream>>>(t0,b,S*D);
  gemm_k<<<dim3(D/16,S/16),tb,0,stream>>>(Cr,S,a,D,1,y1,D,S,D,S);
  gemm_k<<<dim3(D/16,S/16),tb,0,stream>>>(Ci,S,b,D,1,y2,D,S,D,S);
  gemm_k<<<dim3(F/16,S/16),tb,0,stream>>>(xn,D,Wg,F,1,t0,F,S,F,D);
  gemm_k<<<dim3(F/16,S/16),tb,0,stream>>>(xn,D,Wu,F,1,t1,F,S,F,D);
  h_k<<<(S*F+255)/256,256,0,stream>>>(t0,t1,h,S*F);
  gemm_k<<<dim3(D/16,S/16),tb,0,stream>>>(h,F,Wd,D,1,f,D,S,D,F);
  fin_k<<<(S*D+255)/256,256,0,stream>>>(x,y1,y2,f,(u16*)d_out,S*D);
}
```

```cpp
#include <hip/hip_runtime.h>
#include <hip/hip_bf16.h>
#include <stdint.h>

#define LAS __attribute__((address_space(3)))
#define GAS __attribute__((address_space(1)))
typedef unsigned short u16;
typedef short bf16x8 __attribute__((ext_vector_type(8)));
typedef short s16x4 __attribute__((ext_vector_type(4)));
typedef float f32x4 __attribute__((ext_vector_type(4)));
typedef float f32x2 __attribute__((ext_vector_type(2)));
typedef unsigned u32x4 __attribute__((ext_vector_type(4)));
typedef unsigned u32x2 __attribute__((ext_vector_type(2)));
typedef __bf16 bf2_t __attribute__((ext_vector_type(2)));

#define MFMA16(a,b,c) __builtin_amdgcn_mfma_f32_16x16x32_bf16((a),(b),(c),0,0,0)
#define BAR() __builtin_amdgcn_s_barrier()
#define LGKM0() __builtin_amdgcn_s_waitcnt(0xC07F)
#define SGB(m,n) __builtin_amdgcn_sched_group_barrier((m),(n),0)
template<int N> __device__ __forceinline__ void waitv(){ asm volatile("s_waitcnt vmcnt(%0)"::"n"(N):"memory"); }

namespace {
constexpr int S_=2048, D_=448, F_=1792;
constexpr size_t OFF_STATS=0;
constexpr size_t OFF_CS=16384;
constexpr size_t OFF_FLAGS=65536;
constexpr size_t OFF_WDT=262144;
constexpr size_t OFF_H=2u<<20;
constexpr size_t OFF_AB=10u<<20;
constexpr size_t OFF_PART=16u<<20;

__device__ __forceinline__ float bf2f(u16 b){ return __uint_as_float(((unsigned)b)<<16); }
__device__ __forceinline__ float bflo(unsigned u){ return __uint_as_float(u<<16); }
__device__ __forceinline__ float bfhi(unsigned u){ return __uint_as_float(u&0xffff0000u); }
__device__ __forceinline__ unsigned pk2(float a,float b){ f32x2 v={a,b}; bf2_t r=__builtin_convertvector(v,bf2_t); return __builtin_bit_cast(unsigned,r); }
__device__ __forceinline__ float rb(float a){ return bflo(pk2(a,0.f)&0xffffu); }
__device__ __forceinline__ auto mkrsrc(const void* p){ return __builtin_amdgcn_make_buffer_rsrc((void*)p,0,0x7fffffff,0x00020000); }
#define BLDS(rs,ldsp,vo,so) __builtin_amdgcn_raw_ptr_buffer_load_lds((rs),(LAS void*)(ldsp),16,(vo),(so),0,0)

__device__ __forceinline__ void job_stats(const u16* x,float* stats,int row,int lane){
  float v[8]; unsigned w[4]={0,0,0,0};
  if(lane<56){ u32x4 t=*(const u32x4*)(x+(size_t)row*D_+lane*8); w[0]=t[0];w[1]=t[1];w[2]=t[2];w[3]=t[3]; }
  float s=0;
  #pragma unroll
  for(int j=0;j<4;j++){ v[2*j]=bflo(w[j]); v[2*j+1]=bfhi(w[j]); s+=v[2*j]+v[2*j+1]; }
  #pragma unroll
  for(int o=32;o>=1;o>>=1) s+=__shfl_xor(s,o);
  const float m=s*(1.0f/D_);
  float q=0;
  if(lane<56){
    #pragma unroll
    for(int j=0;j<8;j++){ float d=v[j]-m; q+=d*d; }
  }
  #pragma unroll
  for(int o=32;o>=1;o>>=1) q+=__shfl_xor(q,o);
  const float var=q*(1.0f/D_);
  const float mb=rb(m), vb=rb(var);
  const float den=rb(sqrtf(rb(vb+1e-5f)));
  if(lane==0){ stats[row*2]=mb; stats[row*2+1]=1.0f/den; }
}

__global__ __launch_bounds__(256) void k0_prep(const u16* __restrict__ x,const u16* __restrict__ Wr,const u16* __restrict__ Wi,const u16* __restrict__ Wg,const u16* __restrict__ Wu,const u16* __restrict__ Wd,char* __restrict__ ws){
  __shared__ __attribute__((aligned(16))) char sm[32*448*2];
  const int tid=threadIdx.x, wid=tid>>6, lane=tid&63, b=blockIdx.x;
  float* stats=(float*)(ws+OFF_STATS); float* cs=(float*)(ws+OFF_CS);
  #pragma unroll
  for(int r=0;r<2;r++) job_stats(x,stats,b*8+wid*2+r,lane);
  if(tid<4) *(unsigned*)(ws+OFF_FLAGS+(size_t)(b*4+tid)*128)=0u;
  if(b<56){
    const u16* W=(b<28)?Wg:Wu; const int c0=(b%28)*64;
    const int cc=tid&7, rg=tid>>3;
    float a[8]={0,0,0,0,0,0,0,0};
    for(int r=0;r<14;r++){ u32x4 t=*(const u32x4*)(W+(size_t)(rg*14+r)*F_+c0+cc*8);
      #pragma unroll
      for(int j=0;j<4;j++){ a[2*j]+=bflo(t[j]); a[2*j+1]+=bfhi(t[j]); } }
    float* red=(float*)sm;
    #pragma unroll
    for(int j=0;j<8;j++) red[rg*64+cc*8+j]=a[j];
    __syncthreads();
    if(tid<64){ float s=0; for(int r=0;r<32;r++) s+=red[r*64+tid]; cs[(b<28?0:F_)+c0+tid]=s; }
    __syncthreads();
  } else if(b<70){
    const int r0=(b-56)*64;
    for(int r=wid*16;r<wid*16+16;r++){ const int row=r0+r; const u16* W=(row<D_)?(Wr+(size_t)row*D_):(Wi+(size_t)(row-D_)*D_);
      float s=0; if(lane<56){ u32x4 t=*(const u32x4*)(W+lane*8);
        #pragma unroll
        for(int j=0;j<4;j++) s+=bflo(t[j])+bfhi(t[j]); }
      #pragma unroll
      for(int o=32;o>=1;o>>=1) s+=__shfl_xor(s,o);
      if(lane==0) cs[2*F_+row]=s; }
  } else if(b<126){
    const int bb=b-70;
    const u32x4* src=(const u32x4*)(Wd+(size_t)bb*32*D_);
    for(int i=tid;i<32*D_*2/16;i+=256) ((u32x4*)sm)[i]=src[i];
    __syncthreads();
    const u16* t=(const u16*)sm; const int g=lane>>4,i=lane&15;
    for(int et=wid;et<28;et+=4){
      unsigned w[4];
      #pragma unroll
      for(int p=0;p<4;p++){ const int j0=2*p,j1=2*p+1; const int f0=16*(j0>>2)+4*g+(j0&3), f1=16*(j1>>2)+4*g+(j1&3);
        w[p]=(unsigned)t[f0*D_+et*16+i] | ((unsigned)t[f1*D_+et*16+i]<<16); }
      *(u32x4*)(ws+OFF_WDT+((size_t)(bb*28+et))*1024+lane*16)=(u32x4){w[0],w[1],w[2],w[3]};
    }
  }
}

constexpr int S1_SLOT=24576, S1_NS=6;
constexpr int S2_SLOT=49152;

template<int K> struct IC{ static constexpr int v=K; };
template<int A,int B,class Fn> __device__ __forceinline__ void static_for(Fn&& f){ if constexpr(A<B){ f(IC<A>{}); static_for<A+1,B>(f); } }

__host__ __device__ constexpr int s2_after(int jj){ int n=0; if(jj>=2 && (jj&1)==0 && jj/2+2<=6) n+=4; if(jj>=1 && jj+5<=13) n+=4; return n; }
__host__ __device__ constexpr int s2_issued(int j){ int n=36; for(int q=0;q<j;q++) n+=s2_after(q); return n; }
__host__ __device__ constexpr int s2_endGU(int g){ return g<=5 ? 12*(g/2)+8+4*(g&1) : s2_issued(g-5)+s2_after(g-5); }
__host__ __device__ constexpr int s2_endX(int s){ return s<=2 ? 12*s+4 : s2_issued(2*(s-2))+4; }
__host__ __device__ constexpr int s2_need(int j){ int a=s2_endGU(j+1<=13?j+1:13), b=s2_endX((j+1)/2<=6?(j+1)/2:6); return a>b?a:b; }
__device__ __forceinline__ float gelu_mul(float x,float u){
  const float c1=-2.0f*0.7978845608028654f*1.4426950408889634f, c2=c1*0.044715f;
  float z=x*(c1+c2*x*x);
  float e=__builtin_amdgcn_exp2f(z);
  return x*u*__builtin_amdgcn_rcpf(1.0f+e);
}

__global__ __launch_bounds__(512,2) void k1_ffn(const u16* __restrict__ x,const u16* __restrict__ Wr,const u16* __restrict__ Wi,const u16* __restrict__ Wg,const u16* __restrict__ Wu,char* __restrict__ ws){
  extern __shared__ __attribute__((aligned(16))) char smem[];
  LAS char* shm=(LAS char*)smem;
  const int tid=threadIdx.x, wid8=__builtin_amdgcn_readfirstlane(tid>>6), lane=tid&63, wid=wid8&3;
  const int bid=blockIdx.x, xcd=bid&7, idx=bid>>3;
  if(idx>=28) return;
  const int rbk=(xcd>>1)*4+idx/7, cg=(xcd&1)*7+idx%7;
  if(wid8>=4){
    auto rx=mkrsrc((const char*)x+(size_t)rbk*128*896);
    auto rd=mkrsrc((const char*)(cg<7?Wr:Wi)+(size_t)(cg%7)*64*896);
    auto rg=mkrsrc((const char*)Wg+cg*256);
    auto ru=mkrsrc((const char*)Wu+cg*256);
    int xo[4];
    #pragma unroll
    for(int j=0;j<4;j++){ const int r=wid*32+8*j+(lane>>3); const int swz=((lane>>4)&1)|(j<<1); xo[j]=r*896+(((lane&7)^swz)<<4); }
    int dofs[2];
    #pragma unroll
    for(int j=0;j<2;j++){ const int r=wid*16+8*j+(lane>>3); dofs[j]=r*896+(((lane&7)^((r>>1)&7))<<4); }
    int go[2];
    #pragma unroll
    for(int j=0;j<2;j++){ const int r=(wid*2+j)*4+(lane>>4); const int f=(r&3)|(((r>>3)&1)<<2); go[j]=r*3584+(((lane&15)^(f<<1))<<4); }
    auto issueX=[&](int s,LAS char* d){
      #pragma unroll
      for(int j=0;j<4;j++) BLDS(rx,d+(wid*4+j)*1024,xo[j],s*128); };
    auto issueD=[&](int s,LAS char* d){
      #pragma unroll
      for(int j=0;j<2;j++) BLDS(rd,d+(wid*2+j)*1024,dofs[j],s*128); };
    auto issueGU=[&](int j32,LAS char* d){
      #pragma unroll
      for(int j=0;j<2;j++) BLDS(rg,d+(wid*2+j)*1024,go[j],j32*32*3584);
      #pragma unroll
      for(int j=0;j<2;j++) BLDS(ru,d+8192+(wid*2+j)*1024,go[j],j32*32*3584); };
    #pragma unroll
    for(int s=0;s<6;s++){ issueX(s,shm+s*S1_SLOT); issueD(s,shm+s*S1_SLOT+16384); }
    static_for<0,7>([&](auto kc){ constexpr int k=kc.v;
      constexpr int issued=(k>=2)?42:36; waitv<issued-6*(k+1)>(); BAR();
      if constexpr(k==1){ issueX(6,shm); issueD(6,shm+16384); }
    });
    BAR();
    issueX(0,shm); issueGU(0,shm+16384); issueGU(1,shm+32768);
    issueX(1,shm+S2_SLOT); issueGU(2,shm+S2_SLOT+16384); issueGU(3,shm+S2_SLOT+32768);
    issueX(2,shm+2*S2_SLOT); issueGU(4,shm+2*S2_SLOT+16384); issueGU(5,shm+2*S2_SLOT+32768);
    waitv<36-8>(); BAR();
    static_for<0,14>([&](auto jc){ constexpr int j=jc.v;
      constexpr int issued=s2_issued(j), need=s2_need(j);
      waitv<issued-need>(); BAR();
      if constexpr(j>=2 && (j&1)==0 && j/2+2<=6) issueX(j/2+2,shm+((j/2+2)%3)*S2_SLOT);
      if constexpr(j>=1 && j+5<=13) issueGU(j+5,shm+(((j+5)/2)%3)*S2_SLOT+16384+((j+5)&1)*16384);
    });
    return;
  }
  const int wt=wid>>1, wf=wid&1, g=lane>>4, i=lane&15;
  const int xsw=((i>>1)&1)|((i>>2)<<1);
  const unsigned xa0=(wt*64+8*(i>>2)+(i&3))*128+((g^xsw)<<4), xa1=xa0^64;
  const float* stats=(const float*)(ws+OFF_STATS); const float* cs=(const float*)(ws+OFF_CS);
  {
    const unsigned da0=16384+(wf*32+i)*128+((g^((i>>1)&7))<<4), da1=da0^64;
    f32x4 acc[4][2];
    #pragma unroll
    for(int u=0;u<4;u++){ acc[u][0]=(f32x4){0,0,0,0}; acc[u][1]=(f32x4){0,0,0,0}; }
    for(int s=0;s<7;s++){
      BAR();
      LAS char* d=shm+(s==6?0:s)*S1_SLOT;
      #pragma unroll
      for(int kk=0;kk<2;kk++){
        bf16x8 xf[4],df[2];
        #pragma unroll
        for(int u=0;u<4;u++) xf[u]=*(LAS bf16x8*)(d+(kk?xa1:xa0)+(u>>1)*4096+(u&1)*512);
        #pragma unroll
        for(int e=0;e<2;e++) df[e]=*(LAS bf16x8*)(d+(kk?da1:da0)+e*2048);
        #pragma unroll
        for(int u=0;u<4;u++){ acc[u][0]=MFMA16(xf[u],df[0],acc[u][0]); acc[u][1]=MFMA16(xf[u],df[1],acc[u][1]); }
      }
    }
    BAR();
    const float sgn=(cg<7)?1.f:-1.f;
    char* abp=ws+OFF_AB+(size_t)(cg<7?0:64*28)*1024;
    #pragma unroll
    for(int G=0;G<2;G++){
      const int srow=rbk*128+wt*64+G*32+8*g;
      f32x4 st[4];
      #pragma unroll
      for(int p=0;p<4;p++) st[p]=*(const f32x4*)(stats+(srow+2*p)*2);
      #pragma unroll
      for(int e=0;e<2;e++){
        const int ecol=cg*64+wf*32+e*16+i; const float c=cs[2*F_+ecol];
        float v[8];
        #pragma unroll
        for(int n=0;n<2;n++)
        #pragma unroll
        for(int r=0;r<4;r++){ const int j=4*n+r; const float m=st[j>>1][(j&1)*2], ri=st[j>>1][(j&1)*2+1]; v[j]=(acc[2*G+n][e][r]-m*c)*ri*sgn; }
        const int sblk=rbk*4+wt*2+G, et=(cg%7)*4+wf*2+e;
        *(u32x4*)(abp+((size_t)(sblk*28+et))*1024+lane*16)=(u32x4){pk2(v[0],v[1]),pk2(v[2],v[3]),pk2(v[4],v[5]),pk2(v[6],v[7])};
      }
    }
  }
  {
    const int q4=(lane&15)>>2, p4=lane&3, fsw=q4|((g&1)<<2);
    unsigned wa[4];
    #pragma unroll
    for(int j=0;j<4;j++) wa[j]=16384+(8*g+q4)*256+(((4*wf+j)^fsw)<<5)+8*p4;
    f32x4 ag[4][4], au[4][4];
    #pragma unroll
    for(int j=0;j<4;j++)
    #pragma unroll
    for(int u=0;u<4;u++){ ag[j][u]=(f32x4){0,0,0,0}; au[j][u]=(f32x4){0,0,0,0}; }
    auto rdW=[&](LAS char* d,unsigned a)->bf16x8{
      s16x4 lo=__builtin_amdgcn_ds_read_tr16_b64_v4i16((LAS s16x4*)(d+a)), hi=__builtin_amdgcn_ds_read_tr16_b64_v4i16((LAS s16x4*)(d+a+1024));
      return __builtin_shufflevector(lo,hi,0,1,2,3,4,5,6,7); };
    BAR();
    bf16x8 xf[4], w0;
    #pragma unroll
    for(int u=0;u<4;u++) xf[u]=*(LAS bf16x8*)(shm+xa0+(u>>1)*4096+(u&1)*512);
    w0=rdW(shm,wa[0]);
    LGKM0();
    int slot=0;
    for(int s=0;s<7;s++){
      LAS char* d=shm+slot*S2_SLOT; int ns=slot+1; if(ns==3) ns=0; LAS char* dn=shm+ns*S2_SLOT;
      #pragma unroll
      for(int kk=0;kk<2;kk++){
        BAR();
        LAS char* dw=d+kk*16384;
        LAS char* nw=(kk==0)?(d+16384):dn;
        LAS char* nx=(kk==0)?d:dn; const unsigned nxa=(kk==0)?xa1:xa0;
        bf16x8 xn[4];
        bf16x8 wc=w0, wn;
        #pragma unroll
        for(int f=0;f<8;f++){
          const int fn=f+1;
          if(fn<8) wn=rdW(dw,wa[fn>>1]+(fn&1)*8192); else wn=rdW(nw,wa[0]);
          if(f>=4) xn[f-4]=*(LAS bf16x8*)(nx+nxa+((f-4)>>1)*4096+((f-4)&1)*512);
          const int j=f>>1;
          #pragma unroll
          for(int u=0;u<4;u++){ if(f&1) au[j][u]=MFMA16(wc,xf[u],au[j][u]); else ag[j][u]=MFMA16(wc,xf[u],ag[j][u]); }
          wc=wn;
        }
        w0=wc;
        #pragma unroll
        for(int u=0;u<4;u++) xf[u]=xn[u];
        #pragma unroll
        for(int f=0;f<8;f++){ SGB(0x8,1); SGB(0x100,2); SGB(0x8,1); SGB(0x100,1); SGB(0x8,2); }
        __builtin_amdgcn_sched_barrier(0); LGKM0();
      }
      slot=ns;
    }
    f32x2 st[4];
    #pragma unroll
    for(int u=0;u<4;u++){ const int t=rbk*128+wt*64+(u>>1)*32+8*(i>>2)+4*(u&1)+(i&3); st[u]=*(const f32x2*)(stats+t*2); }
    #pragma unroll
    for(int jp=0;jp<2;jp++){
      f32x4 cgv[2],cuv[2];
      #pragma unroll
      for(int jj=0;jj<2;jj++){ const int Fc=cg*128+wf*64+(2*jp+jj)*16+4*g; cgv[jj]=*(const f32x4*)(cs+Fc); cuv[jj]=*(const f32x4*)(cs+F_+Fc); }
      const int blk=cg*4+wf*2+jp;
      #pragma unroll
      for(int u=0;u<4;u++){
        float h[8];
        #pragma unroll
        for(int jj=0;jj<2;jj++)
        #pragma unroll
        for(int r=0;r<4;r++){ const float xg=(ag[2*jp+jj][u][r]-st[u][0]*cgv[jj][r])*st[u][1]; const float xu=(au[2*jp+jj][u][r]-st[u][0]*cuv[jj][r])*st[u][1]; h[4*jj+r]=gelu_mul(xg,xu); }
        const int tf=rbk*8+wt*4+u;
        *(u32x4*)(ws+OFF_H+((size_t)(tf*56+blk))*1024+lane*16)=(u32x4){pk2(h[0],h[1]),pk2(h[2],h[3]),pk2(h[4],h[5]),pk2(h[6],h[7])};
      }
    }
  }
}

constexpr int K2_SLOT=36864, K2_NS=4, K2_NY=16, K2_NF=14, K2_NT=30;
__host__ __device__ constexpr int k2cnt(int st,int w){ return st<K2_NY ? (w<2?8:7) : 9; }
__host__ __device__ constexpr int k2wait(int s,int w){
  int need=(s<K2_NY-1)? s+1 : s; int last=(s+2<K2_NT)? s+2 : K2_NT-1; int n=0; for(int t=need+1;t<=last;t++) n+=k2cnt(t,w); return n; }

__global__ __launch_bounds__(512,2) void k2_dft(const u16* __restrict__ x,const u16* __restrict__ Cr,const u16* __restrict__ Ci,char* __restrict__ ws,u16* __restrict__ out){
  extern __shared__ __attribute__((aligned(16))) char smem[];
  LAS char* shm=(LAS char*)smem;
  const int tid=threadIdx.x, wid8=__builtin_amdgcn_readfirstlane(tid>>6), lane=tid&63, wid=wid8&3;
  const int bid=blockIdx.x, xcd=bid&7, idx=bid>>3, q=xcd>>1, rh=xcd&1, tb=rh*8+(idx>>2), eb=idx&3;
  const int tile=tb*4+eb;
  if(wid8>=4){
    auto rc=mkrsrc((const char*)(q<2?Cr:Ci)+(size_t)(tb*128+wid*32)*4096+(q&1)*2048);
    auto rb_=mkrsrc(ws+OFF_AB+((size_t)((q>>1)*64+(q&1)*32)*28+eb*7)*1024);
    auto rh_=mkrsrc(ws+OFF_H+(size_t)((tb*4+q)*2)*56*1024);
    auto rw=mkrsrc(ws+OFF_WDT+(size_t)(eb*7)*1024);
    int coff[4];
    #pragma unroll
    for(int j=0;j<4;j++){ const int r=8*j+(lane>>3); const int swz=((lane>>4)&1)|(j<<1); coff[j]=r*4096+(((lane&7)^swz)<<4); }
    const int boff=lane*16;
    auto issue=[&](auto sc){ constexpr int s=sc.v;
      LAS char* d=shm+(s%K2_NS)*K2_SLOT;
      if constexpr(s<K2_NY){
        #pragma unroll
        for(int j=0;j<4;j++) BLDS(rc,d+(wid*4+j)*1024,coff[j],s*128);
        #pragma unroll
        for(int j=0;j<4;j++){ const int p=wid+4*j; if(j<3||wid<2){ const int kk=p>=7, et=p-7*kk; BLDS(rb_,d+16384+p*1024,boff,(s*2+kk)*28*1024+et*1024); } }
      } else {
        constexpr int fs=s-K2_NY;
        const int blk=4*fs+wid;
        BLDS(rh_,d+(wid*2+0)*1024,boff,blk*1024);
        BLDS(rh_,d+(wid*2+1)*1024,boff,(56+blk)*1024);
        #pragma unroll
        for(int e=0;e<7;e++) BLDS(rw,d+8192+(wid*7+e)*1024,boff,(blk*28+e)*1024);
      }
    };
    issue(IC<0>{}); issue(IC<1>{}); issue(IC<2>{});
    if(wid<2) waitv<16>(); else waitv<14>();
    BAR();
    static_for<0,K2_NT>([&](auto sc){ constexpr int s=sc.v;
      if(wid<2) waitv<k2wait(s,0)>(); else waitv<k2wait(s,2)>();
      BAR();
      if constexpr(s+3<K2_NT) issue(IC<s+3>{});
    });
    BAR();
    BAR();
    return;
  }
  const int g=lane>>4,i=lane&15;
  const int swz=((i>>1)&1)|((i>>2)<<1);
  const unsigned ca0=(wid*32+8*(i>>2)+(i&3))*128+((g^swz)<<4), ca1=ca0^64;
  const unsigned ba=16384+lane*16;
  f32x4 acc[7][2];
  #pragma unroll
  for(int e=0;e<7;e++){ acc[e][0]=(f32x4){0,0,0,0}; acc[e][1]=(f32x4){0,0,0,0}; }
  BAR();
  bf16x8 c0,c1,b[7];
  c0=*(LAS bf16x8*)(shm+ca0); c1=*(LAS bf16x8*)(shm+ca0+512);
  #pragma unroll
  for(int e=0;e<7;e++) b[e]=*(LAS bf16x8*)(shm+ba+e*1024);
  LGKM0();
  int slot=0;
  for(int s=0;s<K2_NY;s++){
    BAR();
    LAS char* d=shm+slot*K2_SLOT; const int ns=(slot+1)&3; LAS char* dn=shm+ns*K2_SLOT;
    bf16x8 e0=*(LAS bf16x8*)(d+ca1), e1=*(LAS bf16x8*)(d+ca1+512), f[7];
    #pragma unroll
    for(int e=0;e<7;e++) f[e]=*(LAS bf16x8*)(d+ba+(7+e)*1024);
    #pragma unroll
    for(int e=0;e<7;e++){ acc[e][0]=MFMA16(b[e],c0,acc[e][0]); acc[e][1]=MFMA16(b[e],c1,acc[e][1]); }
    c0=*(LAS bf16x8*)(dn+ca0); c1=*(LAS bf16x8*)(dn+ca0+512);
    #pragma unroll
    for(int e=0;e<7;e++) b[e]=*(LAS bf16x8*)(dn+ba+e*1024);
    #pragma unroll
    for(int e=0;e<7;e++){ acc[e][0]=MFMA16(f[e],e0,acc[e][0]); acc[e][1]=MFMA16(f[e],e1,acc[e][1]); }
    #pragma unroll
    for(int h=0;h<2;h++){
      #pragma unroll
      for(int u=0;u<9;u++){ SGB(0x8,1); SGB(0x100,1); }
      SGB(0x8,5); }
    __builtin_amdgcn_sched_barrier(0); LGKM0();
    slot=ns;
  }
  auto rp=__builtin_amdgcn_make_buffer_rsrc((void*)(ws+OFF_PART+(size_t)tile*16*14*1024),0,16*14*1024,0x00020000);
  if(wid!=q){
    #pragma unroll
    for(int e=0;e<7;e++)
    #pragma unroll
    for(int n=0;n<2;n++) __builtin_amdgcn_raw_buffer_store_b128(__builtin_bit_cast(u32x4,acc[e][n]),rp,lane*16,((wid*4+q)*14+e*2+n)*1024,16);
  }
  GAS unsigned* flags=(GAS unsigned*)(ws+OFF_FLAGS);
  const int n_=wid&1, kb=wid>>1;
  f32x4 af[7];
  #pragma unroll
  for(int e=0;e<7;e++) af[e]=(f32x4){0,0,0,0};
  f32x4 pp[4][4]; u32x2 xv[4];
  const int e_lo=kb?4:0, e_n=kb?3:4;
  for(int s=0;s<K2_NF;s++){
    BAR();
    LAS char* d=shm+slot*K2_SLOT;
    #pragma unroll
    for(int h=0;h<2;h++){
      const int bl=kb+2*h;
      bf16x8 hf=*(LAS bf16x8*)(d+(bl*2+n_)*1024+lane*16);
      bf16x8 w[7];
      #pragma unroll
      for(int e=0;e<7;e++) w[e]=*(LAS bf16x8*)(d+8192+(bl*7+e)*1024+lane*16);
      #pragma unroll
      for(int e=0;e<7;e++) af[e]=MFMA16(w[e],hf,af[e]);
    }
    if(s==2){
      waitv<0>();
      if(wid!=q && lane==0) __hip_atomic_store(flags+(size_t)((tile*4+wid)*4+q)*32,1u,__ATOMIC_RELAXED,__HIP_MEMORY_SCOPE_AGENT);
    }
    if(s==10 && wid==0){
      unsigned spins=0;
      for(;;){
        unsigned v=1; if(lane<4 && lane!=q) v=__hip_atomic_load(flags+(size_t)((tile*4+q)*4+lane)*32,__ATOMIC_RELAXED,__HIP_MEMORY_SCOPE_AGENT);
        if(__all(v==1u)) break;
        __builtin_amdgcn_s_sleep(2);
        if(++spins>(1u<<22)) break;
      }
    }
    if(s==11){
      #pragma unroll
      for(int k=0;k<4;k++){
        if(k!=q){
        #pragma unroll
        for(int e=0;e<4;e++) if(e<3||kb==0) pp[k][e]=__builtin_bit_cast(f32x4,__builtin_amdgcn_raw_buffer_load_b128(rp,lane*16,((q*4+k)*14+(e_lo+e)*2+n_)*1024,16)); } }
      const int t=tb*128+q*32+8*(i>>2)+4*n_+(i&3);
      #pragma unroll
      for(int e=0;e<4;e++) if(e<3||kb==0) xv[e]=*(const u32x2*)(x+(size_t)t*D_+eb*112+(e_lo+e)*16+4*g);
    }
    slot=(slot+1)&3;
  }
  BAR();
  if(wid==q){
    #pragma unroll
    for(int e=0;e<7;e++)
    #pragma unroll
    for(int n=0;n<2;n++) *(LAS f32x4*)(shm+(e*2+n)*1024+lane*16)=acc[e][n];
  }
  {
    #pragma unroll
    for(int e=0;e<7;e++){ const bool mine=(kb==0)?(e<4):(e>=4); if(!mine) *(LAS f32x4*)(shm+16384+wid*4096+(kb==0?e-4:e)*1024+lane*16)=af[e]; }
  }
  LGKM0();
  BAR();
  {
    const int pw=wid^2;
    const int t=tb*128+q*32+8*(i>>2)+4*n_+(i&3);
    #pragma unroll
    for(int e=0;e<4;e++) if(e<3||kb==0){
      const int et=e_lo+e;
      f32x4 fo=*(LAS f32x4*)(shm+16384+pw*4096+e*1024+lane*16);
      f32x4 fm=(kb==0)?af[e]:af[(4+e)<7?4+e:6];
      f32x4 yo=*(LAS f32x4*)(shm+(et*2+n_)*1024+lane*16);
      float o[4];
      #pragma unroll
      for(int r=0;r<4;r++){
        float p[4];
        #pragma unroll
        for(int sidx=0;sidx<4;sidx++){ p[sidx]=(sidx==q)?yo[r]:pp[sidx][e][r]; }
        const float y1=rb(p[0]+p[1]), y2=rb(p[2]+p[3]);
        const float sv=rb(y1+y2);
        const float ff=rb(fm[r]+fo[r]);
        const float gg=rb(ff*__builtin_amdgcn_rcpf(1.0f+__builtin_amdgcn_exp2f(-1.4426950408889634f*ff)));
        const float xx=(r&1)?bfhi(xv[e][r>>1]):bflo(xv[e][r>>1]);
        o[r]=xx+rb(sv*gg);
      }
      *(u32x2*)(out+(size_t)t*D_+eb*112+et*16+4*g)=(u32x2){pk2(o[0],o[1]),pk2(o[2],o[3])};
    }
  }
}
}

extern "C" void kernel_launch(void* const* d_in, const int* in_sizes, int n_in, void* d_out, int out_size, void* d_ws, size_t ws_size, hipStream_t stream){
  const u16* x=(const u16*)d_in[0]; const u16* Cr=(const u16*)d_in[1]; const u16* Ci=(const u16*)d_in[2];
  const u16* Wr=(const u16*)d_in[3]; const u16* Wi=(const u16*)d_in[4];
  const u16* Wg=(const u16*)d_in[6]; const u16* Wu=(const u16*)d_in[7]; const u16* Wd=(const u16*)d_in[8];
  char* ws=(char*)d_ws;
  (void)hipFuncSetAttribute((const void*)k1_ffn,hipFuncAttributeMaxDynamicSharedMemorySize,163840);
  (void)hipFuncSetAttribute((const void*)k2_dft,hipFuncAttributeMaxDynamicSharedMemorySize,K2_SLOT*K2_NS);
  k0_prep<<<256,256,0,stream>>>(x,Wr,Wi,Wg,Wu,Wd,ws);
  k1_ffn<<<256,512,163840,stream>>>(x,Wr,Wi,Wg,Wu,ws);
  k2_dft<<<256,512,K2_SLOT*K2_NS,stream>>>(x,Cr,Ci,ws,(u16*)d_out);
}
```

```cpp
#include <hip/hip_runtime.h>
#include <hip/hip_bf16.h>
#include <stdint.h>

#define LAS __attribute__((address_space(3)))
#define GAS __attribute__((address_space(1)))
typedef unsigned short u16;
typedef unsigned long long u64;
typedef short bf16x8 __attribute__((ext_vector_type(8)));
typedef short s16x4 __attribute__((ext_vector_type(4)));
typedef float f32x4 __attribute__((ext_vector_type(4)));
typedef float f32x2 __attribute__((ext_vector_type(2)));
typedef unsigned u32x4 __attribute__((ext_vector_type(4)));
typedef unsigned u32x2 __attribute__((ext_vector_type(2)));
typedef __bf16 bf2_t __attribute__((ext_vector_type(2)));

#define MFMA16(a,b,c) __builtin_amdgcn_mfma_f32_16x16x32_bf16((a),(b),(c),0,0,0)
#define BAR() do{ asm volatile("":::"memory"); __builtin_amdgcn_s_barrier(); asm volatile("":::"memory"); }while(0)
#define LGKM0() __builtin_amdgcn_s_waitcnt(0xC07F)
#define SGB(m,n) __builtin_amdgcn_sched_group_barrier((m),(n),0)
#define SB0() __builtin_amdgcn_sched_barrier(0)
#define RLX_AGENT __ATOMIC_RELAXED,__HIP_MEMORY_SCOPE_AGENT
template<int N> __device__ __forceinline__ void waitv(){ asm volatile("s_waitcnt vmcnt(%0)"::"n"(N):"memory"); }

#define ST_DECL
#define ST(k)
#define ST_INIT()
#define ST_FLUSH(kern,role)

#define ST_AUX 16
namespace {
constexpr int D_=448, F_=1792;
constexpr size_t OFF_G=0;
constexpr size_t OFF_SEEN=4096;
constexpr size_t OFF_HFLAG=8192;
constexpr size_t OFF_STATS=16384;
constexpr size_t OFF_CSD=32768;
constexpr size_t OFF_CSP=131072;
constexpr size_t OFF_FLAGS=65536;
constexpr size_t OFF_WDT=262144;
constexpr size_t OFF_H=2u<<20;
constexpr size_t OFF_AB=10u<<20;
constexpr size_t OFF_PART=16u<<20;
constexpr size_t OFF_ST=40u<<20;
constexpr unsigned SPIN_CAP=1u<<20;

__device__ __forceinline__ float bflo(unsigned u){ return __uint_as_float(u<<16); }
__device__ __forceinline__ float bfhi(unsigned u){ return __uint_as_float(u&0xffff0000u); }
__device__ __forceinline__ unsigned pk2(float a,float b){ f32x2 v={a,b}; bf2_t r=__builtin_convertvector(v,bf2_t); return __builtin_bit_cast(unsigned,r); }
__device__ __forceinline__ float rb(float a){ return bflo(pk2(a,0.f)&0xffffu); }
__device__ __forceinline__ auto mkrsrc(const void* p){ return __builtin_amdgcn_make_buffer_rsrc((void*)p,0,0x7fffffff,0x00020000); }
#define BST16(val,rs,off,aux) __builtin_amdgcn_raw_buffer_store_b128((val),(rs),(off),0,(aux))
#define BLDS(rs,ldsp,vo,so) __builtin_amdgcn_raw_ptr_buffer_load_lds((rs),(LAS void*)(ldsp),16,(vo),(so),0,0)
#define BLDS_SC1(rs,ldsp,vo,so) __builtin_amdgcn_raw_ptr_buffer_load_lds((rs),(LAS void*)(ldsp),16,(vo),(so),0,16)

template<int K> struct IC{ static constexpr int v=K; };
template<int A,int B,class Fn> __device__ __forceinline__ void static_for(Fn&& f){ if constexpr(A<B){ f(IC<A>{}); static_for<A+1,B>(f); } }

__device__ __forceinline__ float sum8(float v){
  v+=__builtin_bit_cast(float,__builtin_amdgcn_update_dpp(0,__builtin_bit_cast(int,v),0xB1,0xF,0xF,true));
  v+=__builtin_bit_cast(float,__builtin_amdgcn_update_dpp(0,__builtin_bit_cast(int,v),0x4E,0xF,0xF,true));
  v+=__builtin_bit_cast(float,__builtin_amdgcn_update_dpp(0,__builtin_bit_cast(int,v),0x141,0xF,0xF,true));
  return v;
}

#define HST(k)
__device__ __forceinline__ void job_stats(const u16* __restrict__ x,char* __restrict__ ws,int row0,int lane){
  const int rr=lane>>3, cc=lane&7;
  const u16* p=x+(size_t)(row0+rr)*D_+cc*8;
  u32x4 xs[7];
  #pragma unroll
  for(int j=0;j<7;j++) xs[j]=*(const u32x4*)(p+j*64);
  f32x4 s4={0,0,0,0};
  #pragma unroll
  for(int j=0;j<7;j++) s4+=(f32x4){bflo(xs[j][0])+bfhi(xs[j][0]),bflo(xs[j][1])+bfhi(xs[j][1]),bflo(xs[j][2])+bfhi(xs[j][2]),bflo(xs[j][3])+bfhi(xs[j][3])};
  const float m=sum8((s4[0]+s4[1])+(s4[2]+s4[3]))*(1.0f/D_);
  f32x4 q4={0,0,0,0};
  #pragma unroll
  for(int j=0;j<7;j++)
  #pragma unroll
  for(int k=0;k<4;k++){ const float a=bflo(xs[j][k])-m, b=bfhi(xs[j][k])-m; q4[k]+=a*a+b*b; }
  const float var=sum8((q4[0]+q4[1])+(q4[2]+q4[3]))*(1.0f/D_);
  const float mb=rb(m), ri=__builtin_amdgcn_rcpf(rb(__builtin_amdgcn_sqrtf(rb(rb(var)+1e-5f))));
  if(cc==0) __hip_atomic_store((GAS u64*)(ws+OFF_STATS)+row0+rr,((u64)__float_as_uint(ri)<<32)|__float_as_uint(mb),RLX_AGENT);
}
__device__ __forceinline__ void job_cs(const u16* __restrict__ Wg,const u16* __restrict__ Wu,char* __restrict__ ws,int c,int sl,int lane){
  const u16* W=(c<14?Wg:Wu)+(c%14)*128+(lane&15)*8+(size_t)(sl*56+(lane>>4))*F_;
  u32x4 t[14];
  #pragma unroll
  for(int r=0;r<14;r++) t[r]=*(const u32x4*)(W+(size_t)(4*r)*F_);
  f32x4 a0={0,0,0,0},a1={0,0,0,0};
  #pragma unroll
  for(int r=0;r<14;r++){ a0+=(f32x4){bflo(t[r][0]),bfhi(t[r][0]),bflo(t[r][1]),bfhi(t[r][1])}; a1+=(f32x4){bflo(t[r][2]),bfhi(t[r][2]),bflo(t[r][3]),bfhi(t[r][3])}; }
  #pragma unroll
  for(int k=0;k<4;k++){ a0[k]+=__shfl_xor(a0[k],16); a1[k]+=__shfl_xor(a1[k],16); }
  #pragma unroll
  for(int k=0;k<4;k++){ a0[k]+=__shfl_xor(a0[k],32); a1[k]+=__shfl_xor(a1[k],32); }
  auto rs=__builtin_amdgcn_make_buffer_rsrc((void*)(ws+OFF_CSP+(size_t)(c*8+sl)*512),0,512,0x00020000);
  const f32x4 v=(lane&16)?a1:a0;
  if(lane<32) BST16(__builtin_bit_cast(u32x4,v),rs,(lane&15)*32+(lane>>4)*16,16);
}
__device__ __forceinline__ void job_csd(const u16* __restrict__ Wr,const u16* __restrict__ Wi,char* __restrict__ ws,int row0,int lane){
  const int rr=(lane>>3)&3, cc=lane&7, row=row0+rr;
  const u16* p=(row<D_?Wr+(size_t)row*D_:Wi+(size_t)(row-D_)*D_)+cc*8;
  u32x4 t[7];
  #pragma unroll
  for(int j=0;j<7;j++) t[j]=*(const u32x4*)(p+j*64);
  float s=0;
  #pragma unroll
  for(int j=0;j<7;j++) s+=((bflo(t[j][0])+bfhi(t[j][0]))+(bflo(t[j][1])+bfhi(t[j][1])))+((bflo(t[j][2])+bfhi(t[j][2]))+(bflo(t[j][3])+bfhi(t[j][3])));
  s=sum8(s);
  if(cc==0 && lane<32) __hip_atomic_store((GAS unsigned*)(ws+OFF_CSD)+row,__float_as_uint(s),RLX_AGENT);
}
__device__ __forceinline__ u64 bcast64(u64 v){ return ((u64)(unsigned)__builtin_amdgcn_readfirstlane((unsigned)(v>>32))<<32)|(unsigned)__builtin_amdgcn_readfirstlane((unsigned)v); }

__device__ __forceinline__ void helper(const u16* __restrict__ x,const u16* __restrict__ Wr,const u16* __restrict__ Wi,const u16* __restrict__ Wg,const u16* __restrict__ Wu,const u16* __restrict__ Wd,char* __restrict__ ws,LAS char* shm,int h){
  const int tid=threadIdx.x, w=__builtin_amdgcn_readfirstlane(tid>>6), lane=tid&63;
  HST(0);
  const u64 g0=__hip_atomic_load((GAS u64*)(ws+OFF_G),RLX_AGENT);
  if(w<7){ const int jb=h*7+w; job_cs(Wg,Wu,ws,jb>>3,jb&7,lane); job_csd(Wr,Wi,ws,h*28+w*4,lane); }
  job_stats(x,ws,h*64+w*8,lane);
  const u64 tag=bcast64(g0)+1;
  HST(2);
  waitv<0>();
  __syncthreads();
  if(tid==0) __hip_atomic_store((GAS u64*)(ws+OFF_HFLAG+h*128),tag,RLX_AGENT);
  HST(3);
  if(tid==0) __hip_atomic_store((GAS u64*)(ws+OFF_SEEN)+blockIdx.x,tag,RLX_AGENT);
  if(tid<24) *(unsigned*)(ws+OFF_FLAGS+(size_t)(h*24+tid)*64)=0u;
  for(int bb=h;bb<56;bb+=32){
    __syncthreads();
    const u32x4* src=(const u32x4*)(Wd+(size_t)bb*32*D_);
    #pragma unroll
    for(int k=0;k<4;k++){ const int c=tid+k*512; if(c<1792) *(LAS u32x4*)(shm+c*16)=src[c]; }
    __syncthreads();
    LAS const u16* t=(LAS const u16*)shm; const int g=lane>>4,i=lane&15;
    for(int et=w;et<28;et+=8){
      unsigned o[4];
      #pragma unroll
      for(int p=0;p<4;p++){ const int j0=2*p,j1=2*p+1; const int f0=16*(j0>>2)+4*g+(j0&3), f1=16*(j1>>2)+4*g+(j1&3);
        o[p]=(unsigned)t[f0*D_+et*16+i] | ((unsigned)t[f1*D_+et*16+i]<<16); }
      *(u32x4*)(ws+OFF_WDT+((size_t)(bb*28+et))*1024+lane*16)=(u32x4){o[0],o[1],o[2],o[3]};
    }
  }
  HST(4);
  if(h==0 && w==0){
    unsigned spins=0;
    for(;;){
      bool ok=true;
      #pragma unroll
      for(int k=0;k<4;k++) ok&=(__hip_atomic_load((GAS u64*)(ws+OFF_SEEN)+k*64+lane,RLX_AGENT)==tag);
      if(__all(ok)) break;
      __builtin_amdgcn_s_sleep(2);
      if(++spins>SPIN_CAP) break;
    }
    if(lane==0) __hip_atomic_store((GAS u64*)(ws+OFF_G),tag,RLX_AGENT);
  }
}

constexpr int XD_SLOT=24576, GU_BASE=73728, GU_SLOT=16384, TB_BASE=2*XD_SLOT;
__host__ __device__ constexpr int k1_after(int j){ int n=0; if(j==0) n+=14; if(j>=2 && (j&1)==0 && j/2+2<=6) n+=6; if(j>=1 && j+4<=13) n+=4; if(j==11) n+=3; return n; }
__host__ __device__ constexpr int k1_issued(int j){ int n=24; for(int q=0;q<j;q++) n+=k1_after(q); return n; }
__host__ __device__ constexpr int k1_endG(int g){ return g==0?10: g==1?14: g==2?24: g==3?28: g==4?38: k1_issued(g-4)+((((g-4)>=2)&&(((g-4)&1)==0)&&((g-4)/2+2<=6))?6:0)+4; }
__host__ __device__ constexpr int k1_endXD(int s){ return s==0?6: s==1?20: s==2?34: k1_issued(2*(s-2))+6; }
__host__ __device__ constexpr int k1_need(int j){ int a=k1_endG(j+1<=13?j+1:13), b=k1_endXD((j+1)/2<=6?(j+1)/2:6); return a>b?a:b; }

__device__ __forceinline__ unsigned geglu2(f32x2 a,f32x2 u,f32x2 cg,f32x2 cu,f32x2 m,f32x2 r){
  const float c1=-2.0f*0.7978845608028654f*1.4426950408889634f, c2=c1*0.044715f;
  const f32x2 x=(a-m*cg)*r, y=(u-m*cu)*r;
  const f32x2 z=x*((x*x)*c2+c1);
  f32x2 e; e.x=__builtin_amdgcn_exp2f(z.x); e.y=__builtin_amdgcn_exp2f(z.y);
  const f32x2 dn=e+1.0f;
  f32x2 rc; rc.x=__builtin_amdgcn_rcpf(dn.x); rc.y=__builtin_amdgcn_rcpf(dn.y);
  const f32x2 h=(x*y)*rc;
  return pk2(h.x,h.y);
}

__global__ __launch_bounds__(512,2) void k1_ffn(const u16* __restrict__ x,const u16* __restrict__ Wr,const u16* __restrict__ Wi,const u16* __restrict__ Wg,const u16* __restrict__ Wu,const u16* __restrict__ Wd,char* __restrict__ ws){
  extern __shared__ __attribute__((aligned(16))) char smem[];
  LAS char* shm=(LAS char*)smem;
  const int tid=threadIdx.x, wid8=__builtin_amdgcn_readfirstlane(tid>>6), lane=tid&63, wid=wid8&3;
  const int bid=blockIdx.x, xcd=bid&7, idx=bid>>3;
  ST_DECL ST_INIT(); ST(0);
  if(idx>=28){
    helper(x,Wr,Wi,Wg,Wu,Wd,ws,shm,xcd*4+(idx-28));
    ST(1); if(wid8==0) ST_FLUSH(0,0);
    return;
  }
  const int rbk=(xcd>>1)*4+idx/7, cg=(xcd&1)*7+idx%7;
  if(wid8>=4){
    auto rx=mkrsrc((const char*)x+(size_t)rbk*128*896);
    auto rd=mkrsrc((const char*)(cg<7?Wr:Wi)+(size_t)(cg%7)*64*896);
    auto rg=mkrsrc((const char*)Wg+cg*256);
    auto ru=mkrsrc((const char*)Wu+cg*256);
    int xo[4];
    #pragma unroll
    for(int j=0;j<4;j++){ const int r=wid*32+8*j+(lane>>3); const int swz=((lane>>4)&1)|(j<<1); xo[j]=r*896+(((lane&7)^swz)<<4); }
    int dofs[2];
    #pragma unroll
    for(int j=0;j<2;j++){ const int r=wid*16+8*j+(lane>>3); dofs[j]=r*896+(((lane&7)^((r>>1)&7))<<4); }
    int go[2];
    #pragma unroll
    for(int j=0;j<2;j++){ const int r=(wid*2+j)*4+(lane>>4); const int f=(r&3)|(((r>>3)&1)<<2); go[j]=r*3584+(((lane&15)^(f<<1))<<4); }
    auto issueXD=[&](int s){ LAS char* d=shm+(s%3)*XD_SLOT;
      #pragma unroll
      for(int j=0;j<4;j++) BLDS(rx,d+(wid*4+j)*1024,xo[j],s*128);
      #pragma unroll
      for(int j=0;j<2;j++) BLDS(rd,d+16384+(wid*2+j)*1024,dofs[j],s*128); };
    auto issueGU=[&](int j32){ LAS char* d=shm+GU_BASE+(j32%5)*GU_SLOT;
      #pragma unroll
      for(int j=0;j<2;j++) BLDS(rg,d+(wid*2+j)*1024,go[j],j32*32*3584);
      #pragma unroll
      for(int j=0;j<2;j++) BLDS(ru,d+8192+(wid*2+j)*1024,go[j],j32*32*3584); };
    u32x4 tv0,tv1,tv2;
    issueXD(0); issueGU(0); issueGU(1); issueXD(1); issueGU(2);
    waitv<14>();
    BAR();
    static_for<0,14>([&](auto jc){ constexpr int j=jc.v;
      waitv<k1_issued(j)-k1_need(j)>();
      BAR();
      if constexpr(j==0){ issueGU(3); issueXD(2); issueGU(4); }
      if constexpr(j>=2 && (j&1)==0 && j/2+2<=6) issueXD(j/2+2);
      if constexpr(j>=1 && j+4<=13) issueGU(j+4);
      if constexpr(j==11){
        auto rs=__builtin_amdgcn_make_buffer_rsrc((void*)ws,0,262144,0x00020000);
        const int gsrc=(int)OFF_CSP+cg*4096, usrc=(int)OFF_CSP+(14+cg)*4096;
        int o0,o1,o2;
        if(wid==0){ o0=(int)OFF_STATS+rbk*1024+lane*16; o1=gsrc+lane*16; o2=gsrc+1024+lane*16; }
        else if(wid==1){ o0=(int)OFF_CSD+cg*256+(lane&15)*16; o1=gsrc+2048+lane*16; o2=gsrc+3072+lane*16; }
        else { o0=usrc+(wid-2)*2048+lane*16; o1=o0+1024; o2=o0; }
        asm volatile("buffer_load_dwordx4 %0, %3, %6, 0 offen sc1\n\tbuffer_load_dwordx4 %1, %4, %6, 0 offen sc1\n\tbuffer_load_dwordx4 %2, %5, %6, 0 offen sc1"
                     :"=&v"(tv0),"=&v"(tv1),"=&v"(tv2):"v"(o0),"v"(o1),"v"(o2),"s"(rs):"memory");
      }
      if constexpr(j==13){
        asm volatile("s_waitcnt vmcnt(0)":"+v"(tv0),"+v"(tv1),"+v"(tv2)::"memory");
        LAS char* tb=shm+TB_BASE+lane*16;
        if(wid==0){ *(LAS u32x4*)(tb)=tv0; *(LAS u32x4*)(tb+2048)=tv1; *(LAS u32x4*)(tb+3072)=tv2; }
        else if(wid==1){ *(LAS u32x4*)(tb+1024)=tv0; *(LAS u32x4*)(tb+4096)=tv1; *(LAS u32x4*)(tb+5120)=tv2; }
        else { *(LAS u32x4*)(tb+6144+(wid-2)*2048)=tv0; *(LAS u32x4*)(tb+7168+(wid-2)*2048)=tv1; }
        LGKM0();
      }
    });
    BAR();
    if(wid==0){
      LAS char* tb=shm+TB_BASE; LAS char* src=tb+2048+(lane>>5)*4096+(lane&31)*16;
      f32x4 s={0,0,0,0};
      #pragma unroll
      for(int r=0;r<8;r++) s+=*(LAS f32x4*)(src+r*512);
      *(LAS f32x4*)(tb+10240+lane*16)=s;
      LGKM0();
    }
    BAR();
    ST(1); if(wid==0) ST_FLUSH(0,1);
    return;
  }
  u64 tag=0;
  if(wid==0){
    tag=bcast64(__hip_atomic_load((GAS u64*)(ws+OFF_G),RLX_AGENT))+1;
    if(lane==0) __hip_atomic_store((GAS u64*)(ws+OFF_SEEN)+bid,tag,RLX_AGENT);
  }
  const int wt=wid>>1, wf=wid&1, g=lane>>4, i=lane&15;
  const int xsw=((i>>1)&1)|((i>>2)<<1);
  const unsigned xa0=(wt*64+8*(i>>2)+(i&3))*128+((g^xsw)<<4), xa1=xa0^64;
  const unsigned da0=16384+(wf*32+i)*128+((g^((i>>1)&7))<<4), da1=da0^64;
  const int q4=(lane&15)>>2, p4=lane&3, fsw=q4|((g&1)<<2);
  unsigned wa[4];
  #pragma unroll
  for(int j=0;j<4;j++) wa[j]=GU_BASE+(8*g+q4)*256+(((4*wf+j)^fsw)<<5)+8*p4;
  f32x4 ag[4][4], au[4][4], aa[4][2];
  #pragma unroll
  for(int u=0;u<4;u++){
    #pragma unroll
    for(int j=0;j<4;j++){ ag[j][u]=(f32x4){0,0,0,0}; au[j][u]=(f32x4){0,0,0,0}; }
    aa[u][0]=(f32x4){0,0,0,0}; aa[u][1]=(f32x4){0,0,0,0}; }
  auto rdW=[&](LAS char* d,unsigned a)->bf16x8{
    s16x4 lo=__builtin_amdgcn_ds_read_tr16_b64_v4i16((LAS s16x4*)(d+a)), hi=__builtin_amdgcn_ds_read_tr16_b64_v4i16((LAS s16x4*)(d+a+1024));
    return __builtin_shufflevector(lo,hi,0,1,2,3,4,5,6,7); };
  BAR();
  ST(1);
  bf16x8 xf[4], w0, w1;
  #pragma unroll
  for(int u=0;u<4;u++) xf[u]=*(LAS bf16x8*)(shm+xa0+(u>>1)*4096+(u&1)*512);
  w0=rdW(shm,wa[0]); w1=rdW(shm,wa[0]+8192);
  LGKM0();
  int xs=0, gs=0;
  u64 hv=tag;
  #pragma unroll 1
  for(int s=0;s<7;s++){
    LAS char* dx=shm+xs*XD_SLOT; const int xn_=(xs==2)?0:xs+1; LAS char* dxn=shm+xn_*XD_SLOT;
    #pragma unroll
    for(int kk=0;kk<2;kk++){
      BAR();
      LAS char* dw=shm+gs*GU_SLOT; const int gn=(gs==4)?0:gs+1; LAS char* nw=shm+gn*GU_SLOT;
      LAS char* nx=(kk==0)?dx:dxn; const unsigned nxa=(kk==0)?xa1:xa0;
      bf16x8 xn[4], df[2];
      bf16x8 wA=w0, wB=w1;
      #pragma unroll
      for(int f=0;f<8;f++){
        const int fn=f+2;
        bf16x8 wC;
        if(fn<8) wC=rdW(dw,wa[fn>>1]+(fn&1)*8192); else wC=rdW(nw,wa[0]+(fn&1)*8192);
        if(f<2) df[f]=*(LAS bf16x8*)(dx+(kk?da1:da0)+f*2048);
        if(f>=4) xn[f-4]=*(LAS bf16x8*)(nx+nxa+((f-4)>>1)*4096+((f-4)&1)*512);
        const int j=f>>1;
        #pragma unroll
        for(int u=0;u<4;u++){ if(f&1) au[j][u]=MFMA16(wA,xf[u],au[j][u]); else ag[j][u]=MFMA16(wA,xf[u],ag[j][u]); }
        wA=wB; wB=wC;
      }
      #pragma unroll
      for(int u=0;u<4;u++){ aa[u][0]=MFMA16(xf[u],df[0],aa[u][0]); aa[u][1]=MFMA16(xf[u],df[1],aa[u][1]); }
      w0=wA; w1=wB;
      #pragma unroll
      for(int u=0;u<4;u++) xf[u]=xn[u];
      #pragma unroll
      for(int f=0;f<2;f++){ SGB(0x8,1); SGB(0x100,1); SGB(0x8,1); SGB(0x100,1); SGB(0x8,1); SGB(0x100,1); SGB(0x8,1); }
      #pragma unroll
      for(int f=0;f<2;f++){ SGB(0x8,1); SGB(0x100,1); SGB(0x8,1); SGB(0x100,1); SGB(0x8,2); }
      #pragma unroll
      for(int f=0;f<4;f++){ SGB(0x8,1); SGB(0x100,1); SGB(0x8,1); SGB(0x100,1); SGB(0x8,1); SGB(0x100,1); SGB(0x8,1); }
      SGB(0x8,8);
      SB0(); LGKM0();
      gs=gn;
      if(wid==0 && s>=4){
        if(kk==1 && s==4){ if(lane<32) hv=__hip_atomic_load((GAS u64*)(ws+OFF_HFLAG+lane*128),RLX_AGENT); }
        if(kk==0 && s==5){
          unsigned spins=0;
          while(!__all(hv==tag)){ __builtin_amdgcn_s_sleep(1); if(lane<32) hv=__hip_atomic_load((GAS u64*)(ws+OFF_HFLAG+lane*128),RLX_AGENT); if(++spins>SPIN_CAP) break; }
        }
      }
    }
    xs=xn_;
  }
  ST(2);
  BAR();
  LAS const float* lst=(LAS const float*)(shm+TB_BASE);
  LAS const float* lcd=(LAS const float*)(shm+TB_BASE+1024);
  LAS const float* lcg=(LAS const float*)(shm+TB_BASE+10240);
  {
    const float sgn=(cg<7)?1.f:-1.f;
    auto rab=__builtin_amdgcn_make_buffer_rsrc((void*)(ws+OFF_AB+(size_t)(cg<7?0:64*28)*1024),0,64*28*1024,0x00020000);
    #pragma unroll
    for(int G=0;G<2;G++){
      const int srow=wt*64+G*32+8*g;
      f32x4 st[4];
      #pragma unroll
      for(int p=0;p<4;p++) st[p]=*(LAS const f32x4*)(lst+(srow+2*p)*2);
      #pragma unroll
      for(int e=0;e<2;e++){
        const float c=lcd[wf*32+e*16+i];
        unsigned w[4];
        #pragma unroll
        for(int p=0;p<4;p++){ const int n=p>>1, r=(p&1)*2;
          const float v0=(aa[2*G+n][e][r]-st[p][0]*c)*(st[p][1]*sgn), v1=(aa[2*G+n][e][r+1]-st[p][2]*c)*(st[p][3]*sgn);
          w[p]=pk2(v0,v1); }
        const int sblk=rbk*4+wt*2+G, et=(cg%7)*4+wf*2+e;
        BST16(((u32x4){w[0],w[1],w[2],w[3]}),rab,(lane*16)+((sblk*28+et)*1024),ST_AUX);
      }
    }
  }
  ST(3);
  BAR();
  {
    auto rhh=__builtin_amdgcn_make_buffer_rsrc((void*)(ws+OFF_H),0,128*56*1024,0x00020000);
    f32x2 st[4];
    #pragma unroll
    for(int u=0;u<4;u++){ const int t=wt*64+(u>>1)*32+8*(i>>2)+4*(u&1)+(i&3); st[u]=*(LAS const f32x2*)(lst+t*2); }
    #pragma unroll
    for(int jp=0;jp<2;jp++){
      f32x4 cgv[2],cuv[2];
      #pragma unroll
      for(int jj=0;jj<2;jj++){ const int Fc=wf*64+(2*jp+jj)*16+4*g; cgv[jj]=*(LAS const f32x4*)(lcg+Fc); cuv[jj]=*(LAS const f32x4*)(lcg+128+Fc); }
      const int blk=cg*4+wf*2+jp;
      #pragma unroll
      for(int u=0;u<4;u++){
        unsigned w[4];
        const f32x2 m2={st[u][0],st[u][0]}, r2={st[u][1],st[u][1]};
        #pragma unroll
        for(int jj=0;jj<2;jj++)
        #pragma unroll
        for(int p=0;p<2;p++){
          const f32x4 ag4=ag[2*jp+jj][u], au4=au[2*jp+jj][u];
          const f32x2 a2={ag4[2*p],ag4[2*p+1]}, u2={au4[2*p],au4[2*p+1]}, cg2={cgv[jj][2*p],cgv[jj][2*p+1]}, cu2={cuv[jj][2*p],cuv[jj][2*p+1]};
          w[2*jj+p]=geglu2(a2,u2,cg2,cu2,m2,r2);
        }
        const int tf=rbk*8+wt*4+u;
        BST16(((u32x4){w[0],w[1],w[2],w[3]}),rhh,(lane*16)+((tf*56+blk)*1024),ST_AUX);
      }
    }
  }
  ST(4);
}

constexpr int K2_SLOT=30720, K2_NS=5;

__global__ __launch_bounds__(512,2) void k2_dft(const u16* __restrict__ x,const u16* __restrict__ Cr,const u16* __restrict__ Ci,char* __restrict__ ws,u16* __restrict__ out){
  extern __shared__ __attribute__((aligned(16))) char smem[];
  LAS char* shm=(LAS char*)smem;
  const int tid=threadIdx.x, wid8=__builtin_amdgcn_readfirstlane(tid>>6), lane=tid&63, wid=wid8&3;
  const int bid=blockIdx.x, xcd=bid&7, idx=bid>>3, role=xcd>>1, rh=xcd&1, tb=rh*8+(idx>>2), eb=idx&3;
  const int tile=tb*4+eb;
  const bool isF=(role==3);
  const int s0=(role==0)?0:(role==1)?22:(role==2)?43:0;
  const int n=(role==0)?22:(role==3)?28:21;
  ST_DECL ST_INIT(); ST(0);
  if(wid8>=4){
    const char* Crb=(const char*)Cr+(size_t)(tb*128+wid*32)*4096;
    const char* Cib=(const char*)Ci+(size_t)(tb*128+wid*32)*4096;
    const char* Hb=ws+OFF_H+(size_t)(tb*8)*56*1024;
    auto rB=mkrsrc((isF?ws+OFF_WDT:ws+OFF_AB)+(size_t)eb*7*1024);
    int va[4], da[4];
    #pragma unroll
    for(int j=0;j<4;j++){
      const int r=8*j+(lane>>3); const int swz=((lane>>4)&1)|(j<<1);
      va[j]=isF ? (((2*wid+(j&1))*56+(j>>1))*1024+lane*16) : (r*4096+(((lane&7)^swz)<<4));
      da[j]=isF ? (((j>>1)*8+2*wid+(j&1))*1024) : ((wid*4+j)*1024);
    }
    const int boff=lane*16;
    auto issue=[&](int s,int slot){
      LAS char* d=shm+slot*K2_SLOT; const int gs=s0+s;
      const char* pa=isF ? Hb+s*2048 : (gs<32 ? Crb+gs*128 : Cib+(gs-32)*128);
      auto rA=mkrsrc(pa);
      #pragma unroll
      for(int j=0;j<4;j++) BLDS(rA,d+da[j],va[j],0);
      #pragma unroll
      for(int j=0;j<4;j++){ const int p=wid+4*j; if(j<3||wid<2){ const int kk=p>=7, et=p-7*kk; BLDS(rB,d+16384+p*1024,boff,((gs*2+kk)*28+et)*1024); } }
    };
    issue(0,0); issue(1,1); issue(2,2);
    if(wid<2) waitv<16>(); else waitv<14>();
    BAR();
    if(wid<2) waitv<8>(); else waitv<7>();
    BAR();
    issue(3,3); issue(4,4);
    int islot=0;
    #pragma unroll 1
    for(int s=1;s<n-4;s++){
      if(wid<2) waitv<16>(); else waitv<14>();
      BAR();
      issue(s+4,islot); islot=(islot==4)?0:islot+1;
    }
    if(!isF){
      if(wid<2) waitv<16>(); else waitv<14>(); BAR();
      if(wid<2) waitv<8>(); else waitv<7>(); BAR();
      waitv<0>(); BAR();
      BAR();
      ST(1);
    } else {
      auto rX=mkrsrc((const char*)x+(size_t)(tb*128+wid*32)*896+eb*224);
      auto rP=__builtin_amdgcn_make_buffer_rsrc((void*)(ws+OFF_PART+(size_t)tile*3*28672),0,3*28672,0x00020000);
      if(wid<2) waitv<16>(); else waitv<14>(); BAR();
      { LAS char* d=shm+3*K2_SLOT+wid*7168;
        #pragma unroll
        for(int j=0;j<7;j++){ const int c=j*64+lane; BLDS(rX,d+j*1024,(c/14)*896+(c%14)*16,0); } }
      if(wid<2) waitv<8+7>(); else waitv<7+7>(); BAR();
      waitv<7>(); BAR();
      BAR();
      GAS unsigned* fl=(GAS unsigned*)(ws+OFF_FLAGS)+(size_t)(tile*12+wid)*16;
      unsigned spins=0;
      for(;;){
        unsigned v=1; if(lane<3) v=__hip_atomic_load(fl+lane*64,RLX_AGENT);
        if(__all(v==1u)) break;
        __builtin_amdgcn_s_sleep(1);
        if(++spins>SPIN_CAP) break;
      }
      asm volatile("":::"memory");
      #pragma unroll
      for(int r=0;r<3;r++){ LAS char* d=shm+((r+4)%5)*K2_SLOT+wid*7168;
        #pragma unroll
        for(int e=0;e<7;e++) BLDS_SC1(rP,d+e*1024,boff,r*28672+wid*7168+e*1024); }
      ST(1);
      waitv<0>(); BAR();
    }
    if(wid==0) ST_FLUSH(1,1);
    return;
  }
  const int g=lane>>4,i=lane&15;
  const int swz=((i>>1)&1)|((i>>2)<<1);
  const unsigned cy=(wid*32+8*(i>>2)+(i&3))*128+((g^swz)<<4);
  const unsigned cf=2*wid*1024+lane*16;
  const unsigned a00=isF?cf:cy, a01=isF?cf+1024:cy+512, a10=isF?cf+8192:(cy^64), a11=isF?cf+8192+1024:((cy^64)+512);
  const unsigned ba=16384+lane*16;
  f32x4 acc[7][2];
  #pragma unroll
  for(int e=0;e<7;e++){ acc[e][0]=(f32x4){0,0,0,0}; acc[e][1]=(f32x4){0,0,0,0}; }
  GAS unsigned* flags=(GAS unsigned*)(ws+OFF_FLAGS);
  BAR();
  ST(1);
  bf16x8 c0,c1,b[7];
  c0=*(LAS bf16x8*)(shm+a00); c1=*(LAS bf16x8*)(shm+a01);
  #pragma unroll
  for(int e=0;e<7;e++) b[e]=*(LAS bf16x8*)(shm+ba+e*1024);
  LGKM0();
  int slot=0;
  #pragma unroll 1
  for(int s=0;s<n;s++){
    BAR();
    LAS char* d=shm+slot*K2_SLOT; const int ns=(slot==4)?0:slot+1; LAS char* dn=shm+ns*K2_SLOT;
    bf16x8 e0=*(LAS bf16x8*)(d+a10), e1=*(LAS bf16x8*)(d+a11), f[7];
    #pragma unroll
    for(int e=0;e<7;e++) f[e]=*(LAS bf16x8*)(d+ba+(7+e)*1024);
    #pragma unroll
    for(int e=0;e<7;e++){ acc[e][0]=MFMA16(b[e],c0,acc[e][0]); acc[e][1]=MFMA16(b[e],c1,acc[e][1]); }
    c0=*(LAS bf16x8*)(dn+a00); c1=*(LAS bf16x8*)(dn+a01);
    #pragma unroll
    for(int e=0;e<7;e++) b[e]=*(LAS bf16x8*)(dn+ba+e*1024);
    #pragma unroll
    for(int e=0;e<7;e++){ acc[e][0]=MFMA16(f[e],e0,acc[e][0]); acc[e][1]=MFMA16(f[e],e1,acc[e][1]); }
    #pragma unroll
    for(int h=0;h<2;h++){
      #pragma unroll
      for(int u=0;u<9;u++){ SGB(0x8,1); SGB(0x100,1); }
      SGB(0x8,5); }
    SB0(); LGKM0();
    slot=ns;
  }
  ST(2);
  if(!isF){
    auto rp=__builtin_amdgcn_make_buffer_rsrc((void*)(ws+OFF_PART+(size_t)(tile*3+role)*28672),0,28672,0x00020000);
    #pragma unroll
    for(int e=0;e<7;e++){
      const u32x4 v={pk2(acc[e][0][0],acc[e][0][1]),pk2(acc[e][0][2],acc[e][0][3]),pk2(acc[e][1][0],acc[e][1][1]),pk2(acc[e][1][2],acc[e][1][3])};
      BST16(v,rp,(lane*16)+(wid*7168+e*1024),16);
    }
    waitv<0>();
    if(lane==0) __hip_atomic_store(flags+(size_t)(tile*12+role*4+wid)*16,1u,RLX_AGENT);
    ST(3); if(wid==0) ST_FLUSH(1,0);
    return;
  }
  #pragma unroll
  for(int e=0;e<7;e++)
  #pragma unroll
  for(int nn=0;nn<2;nn++)
  #pragma unroll
  for(int p=0;p<2;p++){
    const f32x2 ff={acc[e][nn][2*p],acc[e][nn][2*p+1]};
    const f32x2 z=ff*(-1.4426950408889634f);
    f32x2 ex; ex.x=__builtin_amdgcn_exp2f(z.x); ex.y=__builtin_amdgcn_exp2f(z.y);
    const f32x2 dn=ex+1.0f;
    f32x2 rc; rc.x=__builtin_amdgcn_rcpf(dn.x); rc.y=__builtin_amdgcn_rcpf(dn.y);
    const f32x2 gg=ff*rc;
    acc[e][nn][2*p]=gg.x; acc[e][nn][2*p+1]=gg.y;
  }
  ST(3);
  BAR();
  ST(4);
  {
    LAS char* xt=shm+3*K2_SLOT+wid*7168;
    LAS char* p0=shm+4*K2_SLOT+wid*7168+lane*16; LAS char* p1=shm+0*K2_SLOT+wid*7168+lane*16; LAS char* p2=shm+1*K2_SLOT+wid*7168+lane*16;
    LAS char* xp=xt+(8*(i>>2)+(i&3))*224+8*g;
    u32x4 q0[7],q1[7],q2[7]; u32x2 xv[7][2];
    #pragma unroll
    for(int e=0;e<7;e++){ q0[e]=*(LAS u32x4*)(p0+e*1024); q1[e]=*(LAS u32x4*)(p1+e*1024); q2[e]=*(LAS u32x4*)(p2+e*1024);
      xv[e][0]=*(LAS u32x2*)(xp+32*e); xv[e][1]=*(LAS u32x2*)(xp+4*224+32*e); }
    #pragma unroll
    for(int e=0;e<7;e++)
    #pragma unroll
    for(int nn=0;nn<2;nn++){
      unsigned o[2];
      #pragma unroll
      for(int p=0;p<2;p++){
        const unsigned a=q0[e][nn*2+p], bq=q1[e][nn*2+p], c=q2[e][nn*2+p], xx=xv[e][nn][p];
        const f32x2 y=((f32x2){bflo(a),bfhi(a)}+(f32x2){bflo(bq),bfhi(bq)})+(f32x2){bflo(c),bfhi(c)};
        const f32x2 r=y*(f32x2){acc[e][nn][2*p],acc[e][nn][2*p+1]}+(f32x2){bflo(xx),bfhi(xx)};
        o[p]=pk2(r.x,r.y);
      }
      xv[e][nn]=(u32x2){o[0],o[1]};
    }
    #pragma unroll
    for(int e=0;e<7;e++){ *(LAS u32x2*)(xp+32*e)=xv[e][0]; *(LAS u32x2*)(xp+4*224+32*e)=xv[e][1]; }
    LGKM0();
    char* op=(char*)out+(size_t)(tb*128+wid*32)*896+eb*224;
    #pragma unroll
    for(int j=0;j<7;j++){ const int c=j*64+lane; *(u32x4*)(op+(c/14)*896+(c%14)*16)=*(LAS u32x4*)(xt+c*16); }
  }
  ST(5);
}
}

extern "C" void kernel_launch(void* const* d_in, const int* in_sizes, int n_in, void* d_out, int out_size, void* d_ws, size_t ws_size, hipStream_t stream){
  const u16* x=(const u16*)d_in[0]; const u16* Cr=(const u16*)d_in[1]; const u16* Ci=(const u16*)d_in[2];
  const u16* Wr=(const u16*)d_in[3]; const u16* Wi=(const u16*)d_in[4];
  const u16* Wg=(const u16*)d_in[6]; const u16* Wu=(const u16*)d_in[7]; const u16* Wd=(const u16*)d_in[8];
  char* ws=(char*)d_ws;
  (void)hipFuncSetAttribute((const void*)k1_ffn,hipFuncAttributeMaxDynamicSharedMemorySize,163840);
  (void)hipFuncSetAttribute((const void*)k2_dft,hipFuncAttributeMaxDynamicSharedMemorySize,K2_SLOT*K2_NS);
  k1_ffn<<<256,512,163840,stream>>>(x,Wr,Wi,Wg,Wu,Wd,ws);
  k2_dft<<<256,512,K2_SLOT*K2_NS,stream>>>(x,Cr,Ci,ws,(u16*)d_out);
}
```

```cpp
#include <hip/hip_runtime.h>
#include <hip/hip_bf16.h>
#include <stdint.h>

#define LAS __attribute__((address_space(3)))
#define GAS __attribute__((address_space(1)))
typedef unsigned short u16;
typedef unsigned long long u64;
typedef short bf16x8 __attribute__((ext_vector_type(8)));
typedef short s16x4 __attribute__((ext_vector_type(4)));
typedef float f32x4 __attribute__((ext_vector_type(4)));
typedef float f32x2 __attribute__((ext_vector_type(2)));
typedef unsigned u32x4 __attribute__((ext_vector_type(4)));
typedef unsigned u32x2 __attribute__((ext_vector_type(2)));
typedef __bf16 bf2_t __attribute__((ext_vector_type(2)));

#define MFMA16(a,b,c) __builtin_amdgcn_mfma_f32_16x16x32_bf16((a),(b),(c),0,0,0)
#define BAR() do{ asm volatile("":::"memory"); __builtin_amdgcn_s_barrier(); asm volatile("":::"memory"); }while(0)
#define LGKM0() __builtin_amdgcn_s_waitcnt(0xC07F)
#define SGB(m,n) __builtin_amdgcn_sched_group_barrier((m),(n),0)
#define SB0() __builtin_amdgcn_sched_barrier(0)
#define RLX_AGENT __ATOMIC_RELAXED,__HIP_MEMORY_SCOPE_AGENT
template<int N> __device__ __forceinline__ void waitv(){ asm volatile("s_waitcnt vmcnt(%0)"::"n"(N):"memory"); }

#define ST_DECL
#define ST(k)
#define ST_INIT()
#define ST_FLUSH(kern,role)

#define ST_AUX 16
#define SS(v,k)
#define FROT ((tb&7)*2+eb*3)
#define AB_U 1
#define PFN 2
#define PREC 4352
#define PS_ST2 1
#define PS_FLAG 4
#define PS_SAMPLE 8
#define PS_CHECK 10
namespace {
constexpr int D_=448, F_=1792;
constexpr size_t OFF_G=0;
constexpr size_t OFF_SEEN=4096;
constexpr size_t OFF_S1F=114688;
constexpr size_t OFF_S2F=8192;
constexpr size_t OFF_HFA=12288;
constexpr size_t OFF_HFW=36864;
constexpr size_t OFF_STATS=16384;
constexpr size_t OFF_CSD=32768;
constexpr size_t OFF_PF=40960;
constexpr size_t OFF_CSP=131072;
constexpr size_t OFF_WDT=262144;
constexpr size_t OFF_H=2u<<20;
constexpr size_t OFF_AB=10u<<20;
constexpr size_t OFF_PART=16u<<20;
constexpr size_t OFF_ST=40u<<20;
constexpr int CODE_PF_BYTES=36864;
constexpr unsigned SPIN_CAP=1u<<20;

__device__ __forceinline__ float bflo(unsigned u){ return __uint_as_float(u<<16); }
__device__ __forceinline__ float bfhi(unsigned u){ return __uint_as_float(u&0xffff0000u); }
__device__ __forceinline__ unsigned pk2(float a,float b){ f32x2 v={a,b}; bf2_t r=__builtin_convertvector(v,bf2_t); return __builtin_bit_cast(unsigned,r); }
__device__ __forceinline__ float rb(float a){ return bflo(pk2(a,0.f)&0xffffu); }
__device__ __forceinline__ auto mkrsrc(const void* p){ return __builtin_amdgcn_make_buffer_rsrc((void*)p,0,0x7fffffff,0x00020000); }
#define BST16(val,rs,off,aux) __builtin_amdgcn_raw_buffer_store_b128((val),(rs),(off),0,(aux))
#define BLDS(rs,ldsp,vo,so) __builtin_amdgcn_raw_ptr_buffer_load_lds((rs),(LAS void*)(ldsp),16,(vo),(so),0,0)
#define LD_AUX 16
#define BLDS_SC1(rs,ldsp,vo,so) __builtin_amdgcn_raw_ptr_buffer_load_lds((rs),(LAS void*)(ldsp),16,(vo),(so),0,LD_AUX)

template<int K> struct IC{ static constexpr int v=K; };
template<int A,int B,class Fn> __device__ __forceinline__ void static_for(Fn&& f){ if constexpr(A<B){ f(IC<A>{}); static_for<A+1,B>(f); } }

__device__ __forceinline__ float sum8(float v){
  v+=__builtin_bit_cast(float,__builtin_amdgcn_update_dpp(0,__builtin_bit_cast(int,v),0xB1,0xF,0xF,true));
  v+=__builtin_bit_cast(float,__builtin_amdgcn_update_dpp(0,__builtin_bit_cast(int,v),0x4E,0xF,0xF,true));
  v+=__builtin_bit_cast(float,__builtin_amdgcn_update_dpp(0,__builtin_bit_cast(int,v),0x141,0xF,0xF,true));
  return v;
}

__device__ __forceinline__ void job_rows(const u16* __restrict__ x,const u16* __restrict__ Wr,const u16* __restrict__ Wi,char* __restrict__ ws,int row0,int drow0,int lane){
  const int rr=lane>>3, cc=lane&7;
  const u16* p=x+(size_t)(row0+rr)*D_+cc*8;
  const int drow=drow0+(rr&3);
  const u16* pd=(drow<D_?Wr+(size_t)drow*D_:Wi+(size_t)(drow-D_)*D_)+cc*8;
  u32x4 xs[7],t[7];
  #pragma unroll
  for(int j=0;j<7;j++) xs[j]=*(const u32x4*)(p+j*64);
  #pragma unroll
  for(int j=0;j<7;j++) t[j]=*(const u32x4*)(pd+j*64);
  f32x4 s4={0,0,0,0};
  #pragma unroll
  for(int j=0;j<7;j++) s4+=(f32x4){bflo(xs[j][0])+bfhi(xs[j][0]),bflo(xs[j][1])+bfhi(xs[j][1]),bflo(xs[j][2])+bfhi(xs[j][2]),bflo(xs[j][3])+bfhi(xs[j][3])};
  const float m=sum8((s4[0]+s4[1])+(s4[2]+s4[3]))*(1.0f/D_);
  f32x4 q4={0,0,0,0};
  #pragma unroll
  for(int j=0;j<7;j++)
  #pragma unroll
  for(int k=0;k<4;k++){ const float a=bflo(xs[j][k])-m, b=bfhi(xs[j][k])-m; q4[k]+=a*a+b*b; }
  const float var=sum8((q4[0]+q4[1])+(q4[2]+q4[3]))*(1.0f/D_);
  const float mb=rb(m), ri=__builtin_amdgcn_rcpf(rb(__builtin_amdgcn_sqrtf(rb(rb(var)+1e-5f))));
  f32x4 d4={0,0,0,0};
  #pragma unroll
  for(int j=0;j<7;j++) d4+=(f32x4){bflo(t[j][0])+bfhi(t[j][0]),bflo(t[j][1])+bfhi(t[j][1]),bflo(t[j][2])+bfhi(t[j][2]),bflo(t[j][3])+bfhi(t[j][3])};
  const float sd=sum8((d4[0]+d4[1])+(d4[2]+d4[3]));
  if(cc==0){
    __hip_atomic_store((GAS u64*)(ws+OFF_STATS)+row0+rr,((u64)__float_as_uint(ri)<<32)|__float_as_uint(mb),RLX_AGENT);
    if(lane<32 && drow<2*D_) __hip_atomic_store((GAS unsigned*)(ws+OFF_CSD)+drow,__float_as_uint(sd),RLX_AGENT);
  }
}
__device__ __forceinline__ void job_cs(const u16* __restrict__ Wg,const u16* __restrict__ Wu,char* __restrict__ ws,int c,int sl,int lane){
  const u16* W=(c<14?Wg:Wu)+(c%14)*128+(lane&15)*8+(size_t)(sl*56+(lane>>4))*F_;
  u32x4 t[14];
  #pragma unroll
  for(int r=0;r<14;r++) t[r]=*(const u32x4*)(W+(size_t)(4*r)*F_);
  f32x4 a0={0,0,0,0},a1={0,0,0,0};
  #pragma unroll
  for(int r=0;r<14;r++){ a0+=(f32x4){bflo(t[r][0]),bfhi(t[r][0]),bflo(t[r][1]),bfhi(t[r][1])}; a1+=(f32x4){bflo(t[r][2]),bfhi(t[r][2]),bflo(t[r][3]),bfhi(t[r][3])}; }
  #pragma unroll
  for(int k=0;k<4;k++){ a0[k]+=__shfl_xor(a0[k],16); a1[k]+=__shfl_xor(a1[k],16); }
  #pragma unroll
  for(int k=0;k<4;k++){ a0[k]+=__shfl_xor(a0[k],32); a1[k]+=__shfl_xor(a1[k],32); }
  auto rs=__builtin_amdgcn_make_buffer_rsrc((void*)(ws+OFF_CSP+(size_t)(c*8+sl)*512),0,512,0x00020000);
  const f32x4 v=(lane&16)?a1:a0;
  if(lane<32) BST16(__builtin_bit_cast(u32x4,v),rs,(lane&15)*32+(lane>>4)*16,16);
}
__device__ __forceinline__ u64 bcast64(u64 v){ return ((u64)(unsigned)__builtin_amdgcn_readfirstlane((unsigned)(v>>32))<<32)|(unsigned)__builtin_amdgcn_readfirstlane((unsigned)v); }

__device__ __forceinline__ u64 ld64_sc1(const void* p){ u64 v; asm volatile("global_load_dwordx2 %0, %1, off sc1":"=&v"(v):"v"(p):"memory"); return v; }
template<int N> __device__ __forceinline__ void pin64(u64& v){ asm volatile("s_waitcnt vmcnt(%1)":"+v"(v):"n"(N):"memory"); }
__device__ __forceinline__ void spin64(u64 v,const void* p,u64 tag){
  if(__builtin_expect(!__all(v==tag),0)){
    unsigned spins=0;
    for(;;){
      __builtin_amdgcn_s_sleep(1);
      const u64 w=__hip_atomic_load((GAS u64*)p,RLX_AGENT);
      if(__all(w==tag) || ++spins>SPIN_CAP) break;
    }
  }
}

__device__ __forceinline__ void helper(const u16* __restrict__ x,const u16* __restrict__ Wr,const u16* __restrict__ Wi,const u16* __restrict__ Wg,const u16* __restrict__ Wu,const u16* __restrict__ Wd,char* __restrict__ ws,LAS char* shm,int h,u64& tag){
  const int tid=threadIdx.x, w=__builtin_amdgcn_readfirstlane(tid>>6), lane=tid&63;
  const u64 g0=__hip_atomic_load((GAS u64*)(ws+OFF_G),RLX_AGENT);
  if(w<7){ const int jb=h*7+w; job_cs(Wg,Wu,ws,jb>>3,jb&7,lane); }
  job_rows(x,Wr,Wi,ws,h*64+w*8,(w<7)?(h*28+w*4):(h*28+24),lane);
  tag=bcast64(g0)+1;
  waitv<0>();
  __syncthreads();
  if(tid==0){ __hip_atomic_store((GAS u64*)(ws+OFF_HFA+h*128),tag,RLX_AGENT); __hip_atomic_store((GAS u64*)(ws+OFF_SEEN)+blockIdx.x,tag,RLX_AGENT); }
  auto rw=__builtin_amdgcn_make_buffer_rsrc((void*)(ws+OFF_WDT),0,56*28*1024,0x00020000);
  for(int bb=h;bb<56;bb+=32){
    const u32x4* src=(const u32x4*)(Wd+(size_t)bb*32*D_);
    #pragma unroll
    for(int k=0;k<4;k++){ const int c=tid+k*512; if(c<1792) *(LAS u32x4*)(shm+c*16)=src[c]; }
    __syncthreads();
    LAS const u16* t=(LAS const u16*)shm; const int g=lane>>4,i=lane&15;
    for(int et=w;et<28;et+=8){
      unsigned o[4];
      #pragma unroll
      for(int p=0;p<4;p++){ const int j0=2*p,j1=2*p+1; const int f0=16*(j0>>2)+4*g+(j0&3), f1=16*(j1>>2)+4*g+(j1&3);
        o[p]=(unsigned)t[f0*D_+et*16+i] | ((unsigned)t[f1*D_+et*16+i]<<16); }
      BST16(((u32x4){o[0],o[1],o[2],o[3]}),rw,(bb*28+et)*1024+lane*16,16);
    }
    __syncthreads();
  }
  waitv<0>();
  __syncthreads();
  if(tid==0) __hip_atomic_store((GAS u64*)(ws+OFF_HFW+h*128),tag,RLX_AGENT);
  if(h==0 && w==0){
    unsigned spins=0;
    for(;;){
      bool ok=true;
      #pragma unroll
      for(int k=0;k<4;k++) ok&=(__hip_atomic_load((GAS u64*)(ws+OFF_SEEN)+k*64+lane,RLX_AGENT)==tag);
      if(__all(ok)) break;
      __builtin_amdgcn_s_sleep(2);
      if(++spins>SPIN_CAP) break;
    }
    if(lane==0) __hip_atomic_store((GAS u64*)(ws+OFF_G),tag,RLX_AGENT);
  }
}

constexpr int XD_SLOT=24576, GU_BASE=73728, GU_SLOT=16384;
constexpr int TB_BASE=2*XD_SLOT;
constexpr int PF_DUMMY=161792;
constexpr int XQ_BASE=153600;
constexpr int Y_SLOT=30720, F_SLOT=32768;
__host__ __device__ constexpr int k1_after(int j){ int n=0; if(j==0) n+=14; if(j>=2 && (j&1)==0 && j/2+2<=6) n+=6; if(j>=1 && j+4<=13) n+=4; return n; }
__host__ __device__ constexpr int k1_issued(int j){ int n=24; for(int q=0;q<j;q++) n+=k1_after(q); return n; }
__host__ __device__ constexpr int k1_endG(int g){ return g==0?10: g==1?14: g==2?24: g==3?28: g==4?38: k1_issued(g-4)+((((g-4)>=2)&&(((g-4)&1)==0)&&((g-4)/2+2<=6))?6:0)+4; }
__host__ __device__ constexpr int k1_endXD(int s){ return s==0?6: s==1?20: s==2?34: k1_issued(2*(s-2))+6; }
__host__ __device__ constexpr int k1_need(int j){ int a=k1_endG(j+1<=13?j+1:13), b=k1_endXD((j+1)/2<=6?(j+1)/2:6); return a>b?a:b; }

__device__ __forceinline__ unsigned geglu2(f32x2 a,f32x2 u,f32x2 cg,f32x2 cu,f32x2 m,f32x2 r){
  const float c1=-2.0f*0.7978845608028654f*1.4426950408889634f, c2=c1*0.044715f;
  const f32x2 x=(a-m*cg)*r, y=(u-m*cu)*r;
  const f32x2 z=x*((x*x)*c2+c1);
  f32x2 e; e.x=__builtin_amdgcn_exp2f(z.x); e.y=__builtin_amdgcn_exp2f(z.y);
  const f32x2 dn=e+1.0f;
  f32x2 rc; rc.x=__builtin_amdgcn_rcpf(dn.x); rc.y=__builtin_amdgcn_rcpf(dn.y);
  const f32x2 h=(x*y)*rc;
  return pk2(h.x,h.y);
}

template<int CNT>
__device__ __forceinline__ void loader2(const u16* __restrict__ x,const u16* __restrict__ Cr,const u16* __restrict__ Ci,char* __restrict__ ws,LAS char* shm,int wid,int lane,int q,int tb,int eb,u64 tag
  ){
  const int tile=tb*4+eb;
  auto rc=mkrsrc((const char*)(q<2?Cr:Ci)+(size_t)(tb*128+wid*32)*4096+(q&1)*2048);
  auto rb_=mkrsrc(ws+OFF_AB+((size_t)((q>>1)*64+(q&1)*32)*28+eb*7)*1024);
  auto rh_=mkrsrc(ws+OFF_H+(size_t)(tb*8+(q>>1)*4)*56*1024);
  auto rw=mkrsrc(ws+OFF_WDT+(size_t)(eb*7+(q&1)*4)*1024);
  int coff[4];
  #pragma unroll
  for(int j=0;j<4;j++){ const int r=8*j+(lane>>3); const int swz=((lane>>4)&1)|(j<<1); coff[j]=r*4096+(((lane&7)^swz)<<4); }
  const int boff=lane*16;
  const int foff=(FROT)%14;
  auto issueYC=[&](int s,int slot){
    LAS char* d=shm+slot*Y_SLOT;
    #pragma unroll
    for(int j=0;j<4;j++) BLDS(rc,d+(wid*4+j)*1024,coff[j],s*128); };
  auto issueYB=[&](int s,int slot){
    LAS char* d=shm+slot*Y_SLOT;
    #pragma unroll
    for(int j=0;j<CNT-4;j++){ const int p=wid+4*j; const int kk=p>=7, et=p-7*kk; BLDS_SC1(rb_,d+16384+p*1024,boff,((s*2+kk)*28+et)*1024); } };
  auto issueY=[&](int s,int slot){ issueYC(s,slot); issueYB(s,slot); };
  const int w3off=(q&1)?0x7ffffff0:boff;
  auto issueF=[&](int k){
    LAS char* d=shm+((k+1)&3)*F_SLOT; int kr=k+foff; kr=(kr>=14)?kr-14:kr; const int blk=4*kr+wid;
    #pragma unroll
    for(int j=0;j<4;j++) BLDS_SC1(rh_,d+(wid*4+j)*1024,boff,(j*56+blk)*1024);
    #pragma unroll
    for(int e=0;e<4;e++) BLDS_SC1(rw,d+16384+(wid*4+e)*1024,(e<3)?boff:w3off,(blk*28+e)*1024);
  };
  issueYC(0,0); issueYC(1,1); issueYC(2,2);
  {
    const int c0=(eb*112)>>6, c1=(eb*112+111)>>6, rb=(q&1)*8+(lane&7), wv=(lane>>3)&3;
    const int ca=c0+(lane>>5), cb=c1;
    const char* fa=ws+OFF_S1F+(size_t)(((rb*14+(q>>1)*7+ca)*4)+wv)*8; const char* fb=ws+OFF_S1F+(size_t)(((rb*14+(q>>1)*7+cb)*4)+wv)*8;
    LAS volatile unsigned* go=(LAS volatile unsigned*)(shm+PF_DUMMY);
    unsigned spins=0;
    if(wid==0){
      for(;;){
        const u64 va=__hip_atomic_load((GAS u64*)fa,RLX_AGENT), vb=__hip_atomic_load((GAS u64*)fb,RLX_AGENT);
        if(__all(va==tag && vb==tag) || ++spins>SPIN_CAP) break;
        __builtin_amdgcn_s_sleep(1);
      }
      *go=1u;
    } else {
      while(__builtin_amdgcn_readfirstlane(*go)==0u && ++spins<(SPIN_CAP<<4)) __builtin_amdgcn_s_sleep(1);
    }
    asm volatile("":::"memory");
  }
  ST(4);
  issueYB(0,0); issueYB(1,1); issueYB(2,2);
  waitv<2*(CNT-4)>(); BAR();
  SS(tsa,0) waitv<CNT-4>(); SS(tsb,0) BAR(); SS(tsc,0)
  issueY(3,3); issueY(4,4);
  {
    auto rX=__builtin_amdgcn_make_buffer_rsrc((void*)x,0,2048*896,0x00020000);
    #pragma unroll
    for(int j=0;j<2;j++){ const int r=(wid*2+j)*8+(lane>>3); BLDS(rX,shm+XQ_BASE+(wid*2+j)*1024,(tb*128+(q>>1)*64+r)*896+eb*224+(q&1)*128+(lane&7)*16,0); }
  }
  int islot=0;
  #pragma unroll 1
  for(int s=1;s<9;s++){
    SS(tsa,s) waitv<2*CNT>(); SS(tsb,s) BAR(); SS(tsc,s)
    issueY(s+4,islot); islot=(islot==4)?0:islot+1;
  }
  const char* pp=(lane<14)?(ws+OFF_S2F+(size_t)(tb*14+lane)*8):(ws+OFF_HFW+(size_t)(lane&31)*128);
  SS(tsa,9) waitv<2*CNT>(); SS(tsb,9) BAR(); SS(tsc,9)
  u64 pv=tag; if(wid==0) pv=ld64_sc1(pp);
  issueY(13,3);
  SS(tsa,10) waitv<2*CNT>(); SS(tsb,10) BAR(); SS(tsc,10)
  issueY(14,4);
  SS(tsa,11) waitv<2*CNT>(); SS(tsb,11) BAR(); SS(tsc,11)
  issueY(15,0);
  SS(tsa,12) waitv<2*CNT>(); if(wid==0){ pin64<2*CNT>(pv); spin64(pv,pp,tag); } SS(tsb,12) BAR(); SS(tsc,12)
  SS(tsa,13) waitv<CNT>(); SS(tsb,13) BAR(); SS(tsc,13)
  issueF(0);
  SS(tsa,14) waitv<8>(); SS(tsb,14) BAR(); SS(tsc,14)
  issueF(1);
  SS(tsa,15) SS(tsb,15) BAR(); SS(tsc,15)
  issueF(2);
  ST(6);
  const int psrc=(lane>>1)%3; const char* pq=ws+OFF_PF+(size_t)(((tile*4+q)*4+psrc+(psrc>=q?1:0))*2+(lane&1))*32;
  u64 pw=0;
  static_for<0,14>([&](auto kc){ constexpr int k=kc.v;
    constexpr int last=(k+2<=13)?k+2:13;
    SS(tsa,16+k) waitv<8*(last-k)>();
    if constexpr(k==PS_CHECK){ if(wid==0){ pin64<8*((PS_CHECK+2<=13?PS_CHECK+2:13)-(PS_SAMPLE+2))>(pw); spin64(pw,pq,tag); } }
    if constexpr(k==PS_CHECK) ST(5);
    SS(tsb,16+k) BAR(); SS(tsc,16+k)
    if constexpr(k==PS_SAMPLE){ if(wid==0) pw=ld64_sc1(pq); }
    if constexpr(k+3<=13) issueF(k+3);
  });
  ST(7);
  BAR();
  BAR();
}

__global__ __launch_bounds__(512,2) void fnet_geglu(const u16* __restrict__ x,const u16* __restrict__ Cr,const u16* __restrict__ Ci,const u16* __restrict__ Wr,const u16* __restrict__ Wi,
                                                    const u16* __restrict__ Wg,const u16* __restrict__ Wu,const u16* __restrict__ Wd,char* __restrict__ ws,u16* __restrict__ out){
  extern __shared__ __attribute__((aligned(16))) char smem[];
  LAS char* shm=(LAS char*)smem;
  const int tid=threadIdx.x, wid8=__builtin_amdgcn_readfirstlane(tid>>6), lane=tid&63, wid=wid8&3;
  const int bid=blockIdx.x, xcd=bid&7, idx=bid>>3;
  const bool gemm=idx<28;
  const int rbk=(xcd>>1)*4+idx/7, cg=(xcd&1)*7+idx%7, jb=rbk*14+cg;
  const int q=xcd>>1, tb=(xcd&1)*8+(idx>>2), eb=idx&3, tile=tb*4+eb;
  ST_DECL ST_INIT(); ST(0);
  u64 tag=0;
  if(tid==0) *(LAS volatile unsigned*)(shm+PF_DUMMY)=0u;
  if(!gemm && wid8==7){
    auto rpc=__builtin_amdgcn_make_buffer_rsrc((void*)(__builtin_amdgcn_s_getpc()&~127ull),0,CODE_PF_BYTES,0x00020000);
    #pragma unroll
    for(int k=0;k<(CODE_PF_BYTES/128+63)/64;k++) __builtin_amdgcn_raw_ptr_buffer_load_lds(rpc,(LAS void*)(shm+PF_DUMMY+256),4,(k*64+lane)*128,0,0,0);
  }
  if(!gemm && wid8==6){
    auto rws=__builtin_amdgcn_make_buffer_rsrc((void*)ws,0,26u<<20,0x00020000);
    __builtin_amdgcn_raw_ptr_buffer_load_lds(rws,(LAS void*)(shm+PF_DUMMY+1024),4,lane*(512<<10),0,0,0);
    auto rou=__builtin_amdgcn_make_buffer_rsrc((void*)out,0,2048*448*2,0x00020000);
    __builtin_amdgcn_raw_ptr_buffer_load_lds(rou,(LAS void*)(shm+PF_DUMMY+1024),4,lane*(32<<10),0,0,0);
  }
  if(!gemm) helper(x,Wr,Wi,Wg,Wu,Wd,ws,shm,xcd*4+(idx-28),tag);
  if(wid8>=4){
    auto rcp=__builtin_amdgcn_make_buffer_rsrc((void*)((const char*)(q<2?Cr:Ci)+(size_t)(tb*128+eb*32+wid*8)*4096+(q&1)*2048),0,8*4096,0x00020000);
    auto prefetchC=[&](int c0,int nc){ __builtin_amdgcn_raw_ptr_buffer_load_lds(rcp,(LAS void*)(shm+PF_DUMMY+512),4,(lane<8*nc)?((lane&7)*4096+(lane>>3)*128):32768,c0*128,0,0); };
    if(!gemm){ prefetchC(0,8); prefetchC(8,8); }
    if(gemm){
      u64 g0=ld64_sc1(ws+OFF_G);
      auto rx=mkrsrc((const char*)x+(size_t)rbk*128*896);
      auto rd=mkrsrc((const char*)(cg<7?Wr:Wi)+(size_t)(cg%7)*64*896);
      auto rg=mkrsrc((const char*)Wg+cg*256);
      auto ru=mkrsrc((const char*)Wu+cg*256);
      int xo[4];
      #pragma unroll
      for(int j=0;j<4;j++){ const int r=wid*32+8*j+(lane>>3); const int swz=((lane>>4)&1)|(j<<1); xo[j]=r*896+(((lane&7)^swz)<<4); }
      int dofs[2];
      #pragma unroll
      for(int j=0;j<2;j++){ const int r=wid*16+8*j+(lane>>3); dofs[j]=r*896+(((lane&7)^((r>>1)&7))<<4); }
      int go[2];
      #pragma unroll
      for(int j=0;j<2;j++){ const int r=(wid*2+j)*4+(lane>>4); const int f=(r&3)|(((r>>3)&1)<<2); go[j]=r*3584+(((lane&15)^(f<<1))<<4); }
      auto issueXD=[&](int s){ LAS char* d=shm+(s%3)*XD_SLOT;
        #pragma unroll
        for(int j=0;j<4;j++) BLDS(rx,d+(wid*4+j)*1024,xo[j],s*128);
        #pragma unroll
        for(int j=0;j<2;j++) BLDS(rd,d+16384+(wid*2+j)*1024,dofs[j],s*128); };
      auto issueGU=[&](int j32){ LAS char* d=shm+GU_BASE+(j32%5)*GU_SLOT;
        #pragma unroll
        for(int j=0;j<2;j++) BLDS(rg,d+(wid*2+j)*1024,go[j],j32*32*3584);
        #pragma unroll
        for(int j=0;j<2;j++) BLDS(ru,d+8192+(wid*2+j)*1024,go[j],j32*32*3584); };
      auto rs=__builtin_amdgcn_make_buffer_rsrc((void*)ws,0,262144,0x00020000);
      issueXD(0); issueGU(0); issueGU(1); issueXD(1); issueGU(2);
      pin64<24>(g0); tag=bcast64(g0)+1;
      waitv<14>(); BAR();
      u32x4 tv0,tv1,tv2; u64 pv=tag; const char* pa=ws+OFF_HFA+(size_t)(lane&31)*128;
      static_for<0,14>([&](auto jc){ constexpr int j=jc.v;
        SS(tsa,8+j) waitv<k1_issued(j)-k1_need(j)+((j>=11)?3+(j-10):0)>();
        if constexpr(j==10){ if(wid==0){ pin64<14>(pv); spin64(pv,pa,tag); } }
        SS(tsb,8+j) BAR(); SS(tsc,8+j)
        if constexpr(j==0){ issueGU(3); issueXD(2); issueGU(4); }
        if constexpr(j==8){ if(wid==0) pv=ld64_sc1(pa); }
        if constexpr(j>=2 && (j&1)==0 && j/2+2<=6) issueXD(j/2+2);
        if constexpr(j>=1 && j+4<=13) issueGU(j+4);
        if constexpr(j==10){
          const int gsrc=(int)OFF_CSP+cg*4096, usrc=(int)OFF_CSP+(14+cg)*4096;
          int o0,o1,o2;
          if(wid==0){ o0=(int)OFF_STATS+rbk*1024+lane*16; o1=gsrc+lane*16; o2=gsrc+1024+lane*16; }
          else if(wid==1){ o0=(int)OFF_CSD+cg*256+(lane&15)*16; o1=gsrc+2048+lane*16; o2=gsrc+3072+lane*16; }
          else { o0=usrc+(wid-2)*2048+lane*16; o1=o0+1024; o2=o0; }
          asm volatile("buffer_load_dwordx4 %0, %3, %6, 0 offen sc1\n\tbuffer_load_dwordx4 %1, %4, %6, 0 offen sc1\n\tbuffer_load_dwordx4 %2, %5, %6, 0 offen sc1"
                       :"=&v"(tv0),"=&v"(tv1),"=&v"(tv2):"v"(o0),"v"(o1),"v"(o2),"s"(rs):"memory");
        }
        if constexpr(j==12){
          asm volatile("s_waitcnt vmcnt(2)":"+v"(tv0),"+v"(tv1),"+v"(tv2)::"memory");
          LAS char* tb_=shm+TB_BASE+lane*16;
          if(wid==0){ *(LAS u32x4*)(tb_)=tv0; *(LAS u32x4*)(tb_+2048)=tv1; *(LAS u32x4*)(tb_+3072)=tv2; }
          else if(wid==1){ *(LAS u32x4*)(tb_+1024)=tv0; *(LAS u32x4*)(tb_+4096)=tv1; *(LAS u32x4*)(tb_+5120)=tv2; }
          else { *(LAS u32x4*)(tb_+6144+(wid-2)*2048)=tv0; *(LAS u32x4*)(tb_+7168+(wid-2)*2048)=tv1; }
          LGKM0();
        }
        if constexpr(j>=10) prefetchC(PFN*(j-10),PFN);
        if constexpr(j==13){
          if(wid==0){
            LAS char* src=shm+TB_BASE+2048+(lane>>5)*4096+(lane&31)*16;
            f32x4 s={0,0,0,0};
            #pragma unroll
            for(int r=0;r<8;r++) s+=*(LAS f32x4*)(src+r*512);
            *(LAS f32x4*)(shm+TB_BASE+10240+lane*16)=s;
            LGKM0();
          }
        }
      });
      BAR();
      ST(2);
      BAR();
    }
    asm volatile("":::"memory");
    ST(3);
    if(wid<2) loader2<8>(x,Cr,Ci,ws,shm,wid,lane,q,tb,eb,tag); else loader2<7>(x,Cr,Ci,ws,shm,wid,lane,q,tb,eb,tag);
    if(wid==0) ST_FLUSH(0,1);
    return;
  }
  const int g=lane>>4, i=lane&15;
  if(gemm){
    tag=bcast64(__hip_atomic_load((GAS u64*)(ws+OFF_G),RLX_AGENT))+1;
    const int wt=wid>>1, wf=wid&1;
    const int xsw=((i>>1)&1)|((i>>2)<<1);
    const unsigned xa0=(wt*64+8*(i>>2)+(i&3))*128+((g^xsw)<<4), xa1=xa0^64;
    const unsigned da0=16384+(wf*32+i)*128+((g^((i>>1)&7))<<4), da1=da0^64;
    const int q4=(lane&15)>>2, p4=lane&3, fsw=q4|((g&1)<<2);
    unsigned wa[4];
    #pragma unroll
    for(int j=0;j<4;j++) wa[j]=GU_BASE+(8*g+q4)*256+(((4*wf+j)^fsw)<<5)+8*p4;
    f32x4 ag[4][4], au[4][4], aa[4][2];
    #pragma unroll
    for(int u=0;u<4;u++){
      #pragma unroll
      for(int j=0;j<4;j++){ ag[j][u]=(f32x4){0,0,0,0}; au[j][u]=(f32x4){0,0,0,0}; asm volatile("":"+v"(ag[j][u]),"+v"(au[j][u])); }
      aa[u][0]=(f32x4){0,0,0,0}; aa[u][1]=(f32x4){0,0,0,0}; asm volatile("":"+v"(aa[u][0]),"+v"(aa[u][1])); }
    auto rdW=[&](LAS char* d,unsigned a)->bf16x8{
      s16x4 lo=__builtin_amdgcn_ds_read_tr16_b64_v4i16((LAS s16x4*)(d+a)), hi=__builtin_amdgcn_ds_read_tr16_b64_v4i16((LAS s16x4*)(d+a+1024));
      return __builtin_shufflevector(lo,hi,0,1,2,3,4,5,6,7); };
    BAR();
    if(tid==0) __hip_atomic_store((GAS u64*)(ws+OFF_SEEN)+bid,tag,RLX_AGENT);
    ST(1);
    bf16x8 xf[4], w0, w1;
    #pragma unroll
    for(int u=0;u<4;u++) xf[u]=*(LAS bf16x8*)(shm+xa0+(u>>1)*4096+(u&1)*512);
    w0=rdW(shm,wa[0]); w1=rdW(shm,wa[0]+8192);
    LGKM0();
    int xs=0, gs=0;
    #pragma unroll 1
    for(int s=0;s<7;s++){
      LAS char* dx=shm+xs*XD_SLOT; const int xn_=(xs==2)?0:xs+1; LAS char* dxn=shm+xn_*XD_SLOT;
      #pragma unroll
      for(int kk=0;kk<2;kk++){
        BAR();
        LAS char* dw=shm+gs*GU_SLOT; const int gn=(gs==4)?0:gs+1; LAS char* nw=shm+gn*GU_SLOT;
        LAS char* nx=(kk==0)?dx:dxn; const unsigned nxa=(kk==0)?xa1:xa0;
        bf16x8 xn[4], df[2];
        bf16x8 wA=w0, wB=w1;
        #pragma unroll
        for(int f=0;f<8;f++){
          const int fn=f+2;
          bf16x8 wC;
          if(fn<8) wC=rdW(dw,wa[fn>>1]+(fn&1)*8192); else wC=rdW(nw,wa[0]+(fn&1)*8192);
          if(f<2) df[f]=*(LAS bf16x8*)(dx+(kk?da1:da0)+f*2048);
          if(f>=4) xn[f-4]=*(LAS bf16x8*)(nx+nxa+((f-4)>>1)*4096+((f-4)&1)*512);
          const int j=f>>1;
          #pragma unroll
          for(int u=0;u<4;u++){ if(f&1) au[j][u]=MFMA16(wA,xf[u],au[j][u]); else ag[j][u]=MFMA16(wA,xf[u],ag[j][u]); }
          wA=wB; wB=wC;
        }
        #pragma unroll
        for(int u=0;u<4;u++){ aa[u][0]=MFMA16(xf[u],df[0],aa[u][0]); aa[u][1]=MFMA16(xf[u],df[1],aa[u][1]); }
        w0=wA; w1=wB;
        #pragma unroll
        for(int u=0;u<4;u++) xf[u]=xn[u];
        #pragma unroll
        for(int f=0;f<2;f++){ SGB(0x8,1); SGB(0x100,1); SGB(0x8,1); SGB(0x100,1); SGB(0x8,1); SGB(0x100,1); SGB(0x8,1); }
        #pragma unroll
        for(int f=0;f<2;f++){ SGB(0x8,1); SGB(0x100,1); SGB(0x8,1); SGB(0x100,1); SGB(0x8,2); }
        #pragma unroll
        for(int f=0;f<4;f++){ SGB(0x8,1); SGB(0x100,1); SGB(0x8,1); SGB(0x100,1); SGB(0x8,1); SGB(0x100,1); SGB(0x8,1); }
        SGB(0x8,8);
        SB0(); LGKM0();
        gs=gn;
      }
      xs=xn_;
    }
    ST(3);
    {
      int tz=threadIdx.x; asm volatile("":"+v"(tz));
      const int lane=tz&63, g=lane>>4, i=lane&15;
      LAS const float* lst=(LAS const float*)(shm+TB_BASE);
      LAS const float* lcd=(LAS const float*)(shm+TB_BASE+1024);
      LAS const float* lcg=(LAS const float*)(shm+TB_BASE+10240);
      const float sgn=(cg<7)?1.f:-1.f;
      const float c0=lcd[wf*32+i]*sgn, c1=lcd[wf*32+16+i]*sgn;
      auto rab=__builtin_amdgcn_make_buffer_rsrc((void*)(ws+OFF_AB+(size_t)(cg<7?0:64*28)*1024),0,64*28*1024,0x00020000);
      #pragma unroll
      for(int G=0;G<2;G++){
        unsigned w0_[4], w1_[4];
        #pragma unroll
        for(int p=0;p<4;p++){ const int n=p>>1, r=(p&1)*2;
          const f32x4 s4=*(LAS const f32x4*)(lst+(wt*64+G*32+8*g+2*p)*2);
          const float ra=s4[1]*sgn, rb_=s4[3]*sgn, ma=s4[0]*s4[1], mb=s4[2]*s4[3];
          w0_[p]=pk2(aa[2*G+n][0][r]*ra-ma*c0, aa[2*G+n][0][r+1]*rb_-mb*c0);
          w1_[p]=pk2(aa[2*G+n][1][r]*ra-ma*c1, aa[2*G+n][1][r+1]*rb_-mb*c1);
        }
        const int sblk=rbk*4+wt*2+G, et=(cg%7)*4+wf*2;
        BST16(((u32x4){w0_[0],w0_[1],w0_[2],w0_[3]}),rab,(lane*16)+((sblk*28+et)*1024),16);
        BST16(((u32x4){w1_[0],w1_[1],w1_[2],w1_[3]}),rab,(lane*16)+((sblk*28+et+1)*1024),16);
      }
      BAR();
      f32x2 st[4]; f32x4 cgv[4],cuv[4];
      #pragma unroll
      for(int u=0;u<4;u++){ const int t=wt*64+(u>>1)*32+8*(i>>2)+4*(u&1)+(i&3); st[u]=*(LAS const f32x2*)(lst+t*2); }
      #pragma unroll
      for(int j=0;j<4;j++){ const int Fc=wf*64+j*16+4*g; cgv[j]=*(LAS const f32x4*)(lcg+Fc); cuv[j]=*(LAS const f32x4*)(lcg+128+Fc); }
      asm volatile("s_waitcnt lgkmcnt(0)":"+v"(st[0]),"+v"(st[1]),"+v"(st[2]),"+v"(st[3]),"+v"(cgv[0]),"+v"(cgv[1]),"+v"(cgv[2]),"+v"(cgv[3]),"+v"(cuv[0]),"+v"(cuv[1]),"+v"(cuv[2]),"+v"(cuv[3])::"memory");
      BAR();
      ST(2);
      auto rhh=__builtin_amdgcn_make_buffer_rsrc((void*)(ws+OFF_H),0,128*56*1024,0x00020000);
      #pragma unroll
      for(int jp=0;jp<2;jp++){
        const int blk=cg*4+wf*2+jp;
        #pragma unroll
        for(int u=0;u<4;u++){
          unsigned w[4];
          const f32x2 m2={st[u][0],st[u][0]}, r2={st[u][1],st[u][1]};
          #pragma unroll
          for(int jj=0;jj<2;jj++)
          #pragma unroll
          for(int p=0;p<2;p++){
            const f32x4 ag4=ag[2*jp+jj][u], au4=au[2*jp+jj][u], c4=cgv[2*jp+jj], u4=cuv[2*jp+jj];
            const f32x2 a2={ag4[2*p],ag4[2*p+1]}, u2={au4[2*p],au4[2*p+1]}, cg2={c4[2*p],c4[2*p+1]}, cu2={u4[2*p],u4[2*p+1]};
            w[2*jj+p]=geglu2(a2,u2,cg2,cu2,m2,r2);
          }
          const int tf=rbk*8+wt*4+u;
          BST16(((u32x4){w[0],w[1],w[2],w[3]}),rhh,(lane*16)+((tf*56+blk)*1024),16);
          if(jp==0 && u==AB_U){
            waitv<AB_U+1>();
            if(lane==0) __hip_atomic_store((GAS u64*)(ws+OFF_S1F)+jb*4+wid,tag,RLX_AGENT);
          }
        }
      }
      ST(4);
    }
  }
  {
    int tz=threadIdx.x; asm volatile("":"+v"(tz));
    const int lane=tz&63, g=lane>>4, i=lane&15;
    const int swz=((i>>1)&1)|((i>>2)<<1);
    const unsigned ca0=(wid*32+8*(i>>2)+(i&3))*128+((g^swz)<<4), ca1=ca0^64;
    const unsigned ba=16384+lane*16;
    f32x4 acc[7][2];
    #pragma unroll
    for(int e=0;e<7;e++){ acc[e][0]=(f32x4){0,0,0,0}; acc[e][1]=(f32x4){0,0,0,0}; asm volatile("":"+v"(acc[e][0]),"+v"(acc[e][1])); }
    BAR();
    ST(5);
    bf16x8 c0,c1,b[7];
    c0=*(LAS bf16x8*)(shm+ca0); c1=*(LAS bf16x8*)(shm+ca0+512);
    #pragma unroll
    for(int e=0;e<7;e++) b[e]=*(LAS bf16x8*)(shm+ba+e*1024);
    LGKM0();
    int slot=0;
    auto stepy=[&](auto mc){ constexpr int MODE=mc.v;
      BAR();
      if constexpr(MODE==2){ if(gemm && tid==0) __hip_atomic_store((GAS u64*)(ws+OFF_S2F)+jb,tag,RLX_AGENT); }
      LAS char* d=shm+slot*Y_SLOT; const int ns=(slot==4)?0:slot+1; LAS char* dn=shm+ns*Y_SLOT;
      bf16x8 e0=*(LAS bf16x8*)(d+ca1), e1=*(LAS bf16x8*)(d+ca1+512), f[7];
      #pragma unroll
      for(int e=0;e<7;e++) f[e]=*(LAS bf16x8*)(d+ba+(7+e)*1024);
      #pragma unroll
      for(int e=0;e<7;e++){ acc[e][0]=MFMA16(b[e],c0,acc[e][0]); acc[e][1]=MFMA16(b[e],c1,acc[e][1]); }
      c0=*(LAS bf16x8*)(dn+ca0); c1=*(LAS bf16x8*)(dn+ca0+512);
      #pragma unroll
      for(int e=0;e<7;e++) b[e]=*(LAS bf16x8*)(dn+ba+e*1024);
      #pragma unroll
      for(int e=0;e<7;e++){ acc[e][0]=MFMA16(f[e],e0,acc[e][0]); acc[e][1]=MFMA16(f[e],e1,acc[e][1]); }
      #pragma unroll
      for(int h=0;h<2;h++){
        #pragma unroll
        for(int u=0;u<9;u++){ SGB(0x8,1); SGB(0x100,1); }
        SGB(0x8,5); }
      SB0(); LGKM0();
      if constexpr(MODE==1) waitv<0>();
      slot=ns;
    };
    stepy(IC<0>{}); stepy(IC<1>{}); stepy(IC<2>{});
    #pragma unroll 1
    for(int s=3;s<16;s++) stepy(IC<0>{});
    ST(6);
    u32x4 pk[7];
    #pragma unroll
    for(int n=0;n<2;n++)
    #pragma unroll
    for(int p=0;p<3;p++) pk[3*n+p]=(u32x4){pk2(acc[2*p][n][0],acc[2*p][n][1]),pk2(acc[2*p][n][2],acc[2*p][n][3]),pk2(acc[2*p+1][n][0],acc[2*p+1][n][1]),pk2(acc[2*p+1][n][2],acc[2*p+1][n][3])};
    pk[6]=(u32x4){pk2(acc[6][0][0],acc[6][0][1]),pk2(acc[6][0][2],acc[6][0][3]),pk2(acc[6][1][0],acc[6][1][1]),pk2(acc[6][1][2],acc[6][1][3])};
    const int hh=q>>1, eg=q&1;
    const int d0=(wid>>1)*2, d1=d0+1;
    auto rp=__builtin_amdgcn_make_buffer_rsrc((void*)(ws+OFF_PART+(size_t)(tile*32)*PREC),0,32*PREC,0x00020000);
    const int o0=((d0*4+q)*2+(wid&1))*PREC+lane*16, o1=((d1*4+q)*2+(wid&1))*PREC+lane*16;
    const int kb=wid&1, th=wid>>1, n_=wid&1;
    auto fpart=[&](auto nec){ constexpr int NE=nec.v;
      f32x4 af[NE][2];
      #pragma unroll
      for(int e=0;e<NE;e++){ af[e][0]=(f32x4){0,0,0,0}; af[e][1]=(f32x4){0,0,0,0}; }
      u32x4 pin_[3][2];
      const int sa=(NE==4)?(2*n_):n_, sb=(NE==4)?(2*n_+1):2;
      static_for<0,14>([&](auto kc){ constexpr int k=kc.v;
        BAR();
        LAS char* d=shm+((k+1)&3)*F_SLOT;
        bf16x8 hf[2][2], w[2][NE];
        #pragma unroll
        for(int h=0;h<2;h++){
          const int bl=kb+2*h;
          hf[h][0]=*(LAS bf16x8*)(d+(bl*4+th*2+0)*1024+lane*16); hf[h][1]=*(LAS bf16x8*)(d+(bl*4+th*2+1)*1024+lane*16);
          #pragma unroll
          for(int e=0;e<NE;e++) w[h][e]=*(LAS bf16x8*)(d+16384+(bl*4+e)*1024+lane*16);
        }
        if constexpr(k==PS_FLAG){ waitv<0>();
          if(lane==0){
            if(d0!=q) __hip_atomic_store((GAS u64*)(ws+OFF_PF+(size_t)(((tile*4+d0)*4+q)*2+(wid&1))*32),tag,RLX_AGENT);
            if(d1!=q) __hip_atomic_store((GAS u64*)(ws+OFF_PF+(size_t)(((tile*4+d1)*4+q)*2+(wid&1))*32),tag,RLX_AGENT); }
          ST(10); }
        if constexpr(k==PS_CHECK){
          #pragma unroll
          for(int kk=0;kk<3;kk++){ const int src=kk+(kk>=q?1:0); const int o=((q*4+src)*2+th)*PREC+lane*16;
            pin_[kk][0]=__builtin_amdgcn_raw_buffer_load_b128(rp,o+sa*1024,0,16);
            pin_[kk][1]=__builtin_amdgcn_raw_buffer_load_b128(rp,o+sb*1024,0,16); }
        }
        #pragma unroll
        for(int h=0;h<2;h++)
        #pragma unroll
        for(int e=0;e<NE;e++){ af[e][0]=MFMA16(w[h][e],hf[h][0],af[e][0]); af[e][1]=MFMA16(w[h][e],hf[h][1],af[e][1]); }
        SGB(0x100,2+NE);
        #pragma unroll
        for(int u=0;u<2+NE;u++){ SGB(0x100,1); SGB(0x8,1); }
        SGB(0x8,4*NE-2-NE);
        SB0();
        if constexpr(k==0){ if(d0!=q){ BST16(pk[0],rp,o0,16); BST16(pk[1],rp,o0+1024,16); BST16(pk[3],rp,o0+2048,16); BST16(pk[4],rp,o0+3072,16); } }
        if constexpr(k==PS_ST2){ if(d1!=q){ BST16(pk[2],rp,o1,16); BST16(pk[5],rp,o1+1024,16); BST16(pk[6],rp,o1+2048,16); } }
      });
      ST(7);
      BAR();
      if((wid>>1)==hh){
        LAS char* o=shm+(wid&1)*4096+lane*16;
        if(NE==4){ *(LAS u32x4*)(o)=pk[0]; *(LAS u32x4*)(o+1024)=pk[1]; *(LAS u32x4*)(o+2048)=pk[3]; *(LAS u32x4*)(o+3072)=pk[4]; }
        else { *(LAS u32x4*)(o)=pk[2]; *(LAS u32x4*)(o+1024)=pk[5]; *(LAS u32x4*)(o+2048)=pk[6]; }
      }
      #pragma unroll
      for(int e=0;e<NE;e++) *(LAS f32x4*)(shm+8192+wid*4096+e*1024+lane*16)=n_?af[e][0]:af[e][1];
      LGKM0();
      BAR();
      ST(11);
      const int trl=th*32+8*(i>>2)+4*n_+(i&3), trow=tb*128+hh*64+trl;
      u32x4 yo[2];
      yo[0]=*(LAS u32x4*)(shm+th*4096+sa*1024+lane*16); yo[1]=*(LAS u32x4*)(shm+th*4096+sb*1024+lane*16);
      #pragma unroll
      for(int e=0;e<NE;e++){
        const f32x4 fo=*(LAS f32x4*)(shm+8192+(wid^1)*4096+e*1024+lane*16);
        const f32x4 fm=n_?af[e][1]:af[e][0];
        const f32x4 f4=kb?(fo+fm):(fm+fo);
        const u32x2 xv=*(LAS u32x2*)(shm+XQ_BASE+trl*128+e*32+8*g);
        unsigned res[2];
        #pragma unroll
        for(int p=0;p<2;p++){
          auto sel=[&](const u32x4& a,const u32x4& b)->unsigned{ if(e<2) return a[(e&1)*2+p]; if(NE==4) return b[(e&1)*2+p]; return n_?b[2+p]:b[p]; };
          const unsigned y0=sel(yo[0],yo[1]), y1=sel(pin_[0][0],pin_[0][1]), y2=sel(pin_[1][0],pin_[1][1]), y3=sel(pin_[2][0],pin_[2][1]);
          const f32x2 ff={f4[2*p],f4[2*p+1]};
          const f32x2 z=ff*(-1.4426950408889634f);
          f32x2 ex; ex.x=__builtin_amdgcn_exp2f(z.x); ex.y=__builtin_amdgcn_exp2f(z.y);
          const f32x2 dn=ex+1.0f;
          f32x2 rc; rc.x=__builtin_amdgcn_rcpf(dn.x); rc.y=__builtin_amdgcn_rcpf(dn.y);
          const f32x2 y=((f32x2){bflo(y0),bfhi(y0)}+(f32x2){bflo(y1),bfhi(y1)})+((f32x2){bflo(y2),bfhi(y2)}+(f32x2){bflo(y3),bfhi(y3)});
          const f32x2 r=y*(ff*rc)+(f32x2){bflo(xv[p]),bfhi(xv[p])};
          res[p]=pk2(r.x,r.y);
        }
        *(u32x2*)(out+(size_t)trow*D_+eb*112+(4*eg+e)*16+4*g)=(u32x2){res[0],res[1]};
      }
    };
    if(eg==0) fpart(IC<4>{}); else fpart(IC<3>{});
    ST(8);
  }
}
}

extern "C" void kernel_launch(void* const* d_in, const int* in_sizes, int n_in, void* d_out, int out_size, void* d_ws, size_t ws_size, hipStream_t stream){
  const u16* x=(const u16*)d_in[0]; const u16* Cr=(const u16*)d_in[1]; const u16* Ci=(const u16*)d_in[2];
  const u16* Wr=(const u16*)d_in[3]; const u16* Wi=(const u16*)d_in[4];
  const u16* Wg=(const u16*)d_in[6]; const u16* Wu=(const u16*)d_in[7]; const u16* Wd=(const u16*)d_in[8];
  (void)hipFuncSetAttribute((const void*)fnet_geglu,hipFuncAttributeMaxDynamicSharedMemorySize,163840);
  fnet_geglu<<<256,512,163840,stream>>>(x,Cr,Ci,Wr,Wi,Wg,Wu,Wd,(char*)d_ws,(u16*)d_out);
}
```

```cpp
#include <hip/hip_runtime.h>
#include <hip/hip_bf16.h>
#include <stdint.h>

#define LAS __attribute__((address_space(3)))
#define GAS __attribute__((address_space(1)))
typedef unsigned short u16;
typedef unsigned long long u64;
typedef short bf16x8 __attribute__((ext_vector_type(8)));
typedef short s16x4 __attribute__((ext_vector_type(4)));
typedef float f32x4 __attribute__((ext_vector_type(4)));
typedef float f32x2 __attribute__((ext_vector_type(2)));
typedef unsigned u32x4 __attribute__((ext_vector_type(4)));
typedef unsigned u32x2 __attribute__((ext_vector_type(2)));
typedef __bf16 bf2_t __attribute__((ext_vector_type(2)));

#define MFMA16(a,b,c) __builtin_amdgcn_mfma_f32_16x16x32_bf16((a),(b),(c),0,0,0)
#define BAR() do{ asm volatile("":::"memory"); __builtin_amdgcn_s_barrier(); asm volatile("":::"memory"); }while(0)
#define LGKM0() __builtin_amdgcn_s_waitcnt(0xC07F)
#define SGB(m,n) __builtin_amdgcn_sched_group_barrier((m),(n),0)
#define SB0() __builtin_amdgcn_sched_barrier(0)
#define RLX_AGENT __ATOMIC_RELAXED,__HIP_MEMORY_SCOPE_AGENT
template<int N> __device__ __forceinline__ void waitv(){ asm volatile("s_waitcnt vmcnt(%0)"::"n"(N):"memory"); }

namespace {
constexpr int D_=448, F_=1792;
constexpr int AB_U=1;
constexpr int PFN=2;
constexpr int PREC=4352;
constexpr int PS_ST2=1, PS_FLAG=4, PS_SAMPLE=8, PS_CHECK=10;
constexpr size_t OFF_G=0;
constexpr size_t OFF_SEEN=4096;
constexpr size_t OFF_S1F=114688;
constexpr size_t OFF_S2F=8192;
constexpr size_t OFF_HFA=12288;
constexpr size_t OFF_HFW=36864;
constexpr size_t OFF_STATS=16384;
constexpr size_t OFF_CSD=32768;
constexpr size_t OFF_PF=40960;
constexpr size_t OFF_CSP=131072;
constexpr size_t OFF_WDT=262144;
constexpr size_t OFF_H=2u<<20;
constexpr size_t OFF_AB=10u<<20;
constexpr size_t OFF_PART=16u<<20;
constexpr int CODE_PF_BYTES=36864;
constexpr unsigned SPIN_CAP=1u<<17;

__device__ __forceinline__ float bflo(unsigned u){ return __uint_as_float(u<<16); }
__device__ __forceinline__ float bfhi(unsigned u){ return __uint_as_float(u&0xffff0000u); }
__device__ __forceinline__ unsigned pk2(float a,float b){ f32x2 v={a,b}; bf2_t r=__builtin_convertvector(v,bf2_t); return __builtin_bit_cast(unsigned,r); }
__device__ __forceinline__ float rb(float a){ return bflo(pk2(a,0.f)&0xffffu); }
__device__ __forceinline__ auto mkrsrc(const void* p){ return __builtin_amdgcn_make_buffer_rsrc((void*)p,0,0x7fffffff,0x00020000); }
#define BST16(val,rs,off,aux) __builtin_amdgcn_raw_buffer_store_b128((val),(rs),(off),0,(aux))
#define BLDS(rs,ldsp,vo,so) __builtin_amdgcn_raw_ptr_buffer_load_lds((rs),(LAS void*)(ldsp),16,(vo),(so),0,0)
#define BLDS_SC1(rs,ldsp,vo,so) __builtin_amdgcn_raw_ptr_buffer_load_lds((rs),(LAS void*)(ldsp),16,(vo),(so),0,16)

template<int K> struct IC{ static constexpr int v=K; };
template<int A,int B,class Fn> __device__ __forceinline__ void static_for(Fn&& f){ if constexpr(A<B){ f(IC<A>{}); static_for<A+1,B>(f); } }

__device__ __forceinline__ float sum8(float v){
  v+=__builtin_bit_cast(float,__builtin_amdgcn_update_dpp(0,__builtin_bit_cast(int,v),0xB1,0xF,0xF,true));
  v+=__builtin_bit_cast(float,__builtin_amdgcn_update_dpp(0,__builtin_bit_cast(int,v),0x4E,0xF,0xF,true));
  v+=__builtin_bit_cast(float,__builtin_amdgcn_update_dpp(0,__builtin_bit_cast(int,v),0x141,0xF,0xF,true));
  return v;
}

__device__ __forceinline__ void job_rows(const u16* __restrict__ x,const u16* __restrict__ Wr,const u16* __restrict__ Wi,char* __restrict__ ws,int row0,int drow0,int lane){
  const int rr=lane>>3, cc=lane&7;
  const u16* p=x+(size_t)(row0+rr)*D_+cc*8;
  const int drow=drow0+(rr&3);
  const u16* pd=(drow<D_?Wr+(size_t)drow*D_:Wi+(size_t)(drow-D_)*D_)+cc*8;
  u32x4 xs[7],t[7];
  #pragma unroll
  for(int j=0;j<7;j++) xs[j]=*(const u32x4*)(p+j*64);
  #pragma unroll
  for(int j=0;j<7;j++) t[j]=*(const u32x4*)(pd+j*64);
  f32x4 s4={0,0,0,0};
  #pragma unroll
  for(int j=0;j<7;j++) s4+=(f32x4){bflo(xs[j][0])+bfhi(xs[j][0]),bflo(xs[j][1])+bfhi(xs[j][1]),bflo(xs[j][2])+bfhi(xs[j][2]),bflo(xs[j][3])+bfhi(xs[j][3])};
  const float m=sum8((s4[0]+s4[1])+(s4[2]+s4[3]))*(1.0f/D_);
  f32x4 q4={0,0,0,0};
  #pragma unroll
  for(int j=0;j<7;j++)
  #pragma unroll
  for(int k=0;k<4;k++){ const float a=bflo(xs[j][k])-m, b=bfhi(xs[j][k])-m; q4[k]+=a*a+b*b; }
  const float var=sum8((q4[0]+q4[1])+(q4[2]+q4[3]))*(1.0f/D_);
  const float mb=rb(m), ri=__builtin_amdgcn_rcpf(rb(__builtin_amdgcn_sqrtf(rb(rb(var)+1e-5f))));
  f32x4 d4={0,0,0,0};
  #pragma unroll
  for(int j=0;j<7;j++) d4+=(f32x4){bflo(t[j][0])+bfhi(t[j][0]),bflo(t[j][1])+bfhi(t[j][1]),bflo(t[j][2])+bfhi(t[j][2]),bflo(t[j][3])+bfhi(t[j][3])};
  const float sd=sum8((d4[0]+d4[1])+(d4[2]+d4[3]));
  if(cc==0){
    __hip_atomic_store((GAS u64*)(ws+OFF_STATS)+row0+rr,((u64)__float_as_uint(ri)<<32)|__float_as_uint(mb),RLX_AGENT);
    if(lane<32 && drow<2*D_) __hip_atomic_store((GAS unsigned*)(ws+OFF_CSD)+drow,__float_as_uint(sd),RLX_AGENT);
  }
}
__device__ __forceinline__ void job_cs(const u16* __restrict__ Wg,const u16* __restrict__ Wu,char* __restrict__ ws,int c,int sl,int lane){
  const u16* W=(c<14?Wg:Wu)+(c%14)*128+(lane&15)*8+(size_t)(sl*56+(lane>>4))*F_;
  u32x4 t[14];
  #pragma unroll
  for(int r=0;r<14;r++) t[r]=*(const u32x4*)(W+(size_t)(4*r)*F_);
  f32x4 a0={0,0,0,0},a1={0,0,0,0};
  #pragma unroll
  for(int r=0;r<14;r++){ a0+=(f32x4){bflo(t[r][0]),bfhi(t[r][0]),bflo(t[r][1]),bfhi(t[r][1])}; a1+=(f32x4){bflo(t[r][2]),bfhi(t[r][2]),bflo(t[r][3]),bfhi(t[r][3])}; }
  #pragma unroll
  for(int k=0;k<4;k++){ a0[k]+=__shfl_xor(a0[k],16); a1[k]+=__shfl_xor(a1[k],16); }
  #pragma unroll
  for(int k=0;k<4;k++){ a0[k]+=__shfl_xor(a0[k],32); a1[k]+=__shfl_xor(a1[k],32); }
  auto rs=__builtin_amdgcn_make_buffer_rsrc((void*)(ws+OFF_CSP+(size_t)(c*8+sl)*512),0,512,0x00020000);
  const f32x4 v=(lane&16)?a1:a0;
  if(lane<32) BST16(__builtin_bit_cast(u32x4,v),rs,(lane&15)*32+(lane>>4)*16,16);
}
__device__ __forceinline__ u64 bcast64(u64 v){ return ((u64)(unsigned)__builtin_amdgcn_readfirstlane((unsigned)(v>>32))<<32)|(unsigned)__builtin_amdgcn_readfirstlane((unsigned)v); }

__device__ __forceinline__ u64 ld64_sc1(const void* p){ u64 v; asm volatile("global_load_dwordx2 %0, %1, off sc1":"=&v"(v):"v"(p):"memory"); return v; }
template<int N> __device__ __forceinline__ void pin64(u64& v){ asm volatile("s_waitcnt vmcnt(%1)":"+v"(v):"n"(N):"memory"); }
__device__ __forceinline__ void spin64(u64 v,const void* p,u64 tag){
  if(__builtin_expect(!__all(v==tag),0)){
    unsigned spins=0;
    for(;;){
      __builtin_amdgcn_s_sleep(1);
      const u64 w=__hip_atomic_load((GAS u64*)p,RLX_AGENT);
      if(__all(w==tag) || ++spins>SPIN_CAP) break;
    }
  }
}

__device__ __forceinline__ void helper(const u16* __restrict__ x,const u16* __restrict__ Wr,const u16* __restrict__ Wi,const u16* __restrict__ Wg,const u16* __restrict__ Wu,const u16* __restrict__ Wd,char* __restrict__ ws,LAS char* shm,int h,u64& tag){
  const int tid=threadIdx.x, w=__builtin_amdgcn_readfirstlane(tid>>6), lane=tid&63;
  const u64 g0=__hip_atomic_load((GAS u64*)(ws+OFF_G),RLX_AGENT);
  if(w<7){ const int jb=h*7+w; job_cs(Wg,Wu,ws,jb>>3,jb&7,lane); }
  job_rows(x,Wr,Wi,ws,h*64+w*8,(w<7)?(h*28+w*4):(h*28+24),lane);
  tag=bcast64(g0)+1;
  waitv<0>();
  __syncthreads();
  if(tid==0){ __hip_atomic_store((GAS u64*)(ws+OFF_HFA+h*128),tag,RLX_AGENT); __hip_atomic_store((GAS u64*)(ws+OFF_SEEN)+blockIdx.x,tag,RLX_AGENT); }
  auto rw=__builtin_amdgcn_make_buffer_rsrc((void*)(ws+OFF_WDT),0,56*28*1024,0x00020000);
  for(int bb=h;bb<56;bb+=32){
    const u32x4* src=(const u32x4*)(Wd+(size_t)bb*32*D_);
    #pragma unroll
    for(int k=0;k<4;k++){ const int c=tid+k*512; if(c<1792) *(LAS u32x4*)(shm+c*16)=src[c]; }
    __syncthreads();
    LAS const u16* t=(LAS const u16*)shm; const int g=lane>>4,i=lane&15;
    for(int et=w;et<28;et+=8){
      unsigned o[4];
      #pragma unroll
      for(int p=0;p<4;p++){ const int j0=2*p,j1=2*p+1; const int f0=16*(j0>>2)+4*g+(j0&3), f1=16*(j1>>2)+4*g+(j1&3);
        o[p]=(unsigned)t[f0*D_+et*16+i] | ((unsigned)t[f1*D_+et*16+i]<<16); }
      BST16(((u32x4){o[0],o[1],o[2],o[3]}),rw,(bb*28+et)*1024+lane*16,16);
    }
    __syncthreads();
  }
  waitv<0>();
  __syncthreads();
  if(tid==0) __hip_atomic_store((GAS u64*)(ws+OFF_HFW+h*128),tag,RLX_AGENT);
  if(h==0 && w==0){
    unsigned spins=0;
    for(;;){
      bool ok=true;
      #pragma unroll
      for(int k=0;k<4;k++) ok&=(__hip_atomic_load((GAS u64*)(ws+OFF_SEEN)+k*64+lane,RLX_AGENT)==tag);
      if(__all(ok)) break;
      __builtin_amdgcn_s_sleep(2);
      if(++spins>SPIN_CAP) break;
    }
    if(lane==0) __hip_atomic_store((GAS u64*)(ws+OFF_G),tag,RLX_AGENT);
  }
}

constexpr int XD_SLOT=24576, GU_BASE=73728, GU_SLOT=16384;
constexpr int TB_BASE=2*XD_SLOT;
constexpr int PF_DUMMY=161792;
constexpr int XQ_BASE=153600;
constexpr int Y_SLOT=30720, F_SLOT=32768;
__host__ __device__ constexpr int k1_after(int j){ int n=0; if(j==0) n+=14; if(j>=2 && (j&1)==0 && j/2+2<=6) n+=6; if(j>=1 && j+4<=13) n+=4; return n; }
__host__ __device__ constexpr int k1_issued(int j){ int n=24; for(int q=0;q<j;q++) n+=k1_after(q); return n; }
__host__ __device__ constexpr int k1_endG(int g){ return g==0?10: g==1?14: g==2?24: g==3?28: g==4?38: k1_issued(g-4)+((((g-4)>=2)&&(((g-4)&1)==0)&&((g-4)/2+2<=6))?6:0)+4; }
__host__ __device__ constexpr int k1_endXD(int s){ return s==0?6: s==1?20: s==2?34: k1_issued(2*(s-2))+6; }
__host__ __device__ constexpr int k1_need(int j){ int a=k1_endG(j+1<=13?j+1:13), b=k1_endXD((j+1)/2<=6?(j+1)/2:6); return a>b?a:b; }

__device__ __forceinline__ unsigned geglu2(f32x2 a,f32x2 u,f32x2 cg,f32x2 cu,f32x2 m,f32x2 r){
  const float c1=-2.0f*0.7978845608028654f*1.4426950408889634f, c2=c1*0.044715f;
  const f32x2 x=(a-m*cg)*r, y=(u-m*cu)*r;
  const f32x2 z=x*((x*x)*c2+c1);
  f32x2 e; e.x=__builtin_amdgcn_exp2f(z.x); e.y=__builtin_amdgcn_exp2f(z.y);
  const f32x2 dn=e+1.0f;
  f32x2 rc; rc.x=__builtin_amdgcn_rcpf(dn.x); rc.y=__builtin_amdgcn_rcpf(dn.y);
  const f32x2 h=(x*y)*rc;
  return pk2(h.x,h.y);
}

template<int CNT>
__device__ __forceinline__ void loader2(const u16* __restrict__ x,const u16* __restrict__ Cr,const u16* __restrict__ Ci,char* __restrict__ ws,LAS char* shm,int wid,int lane,int q,int tb,int eb,u64 tag
  ){
  const int tile=tb*4+eb;
  auto rc=mkrsrc((const char*)(q<2?Cr:Ci)+(size_t)(tb*128+wid*32)*4096+(q&1)*2048);
  auto rb_=mkrsrc(ws+OFF_AB+((size_t)((q>>1)*64+(q&1)*32)*28+eb*7)*1024);
  auto rh_=mkrsrc(ws+OFF_H+(size_t)(tb*8+(q>>1)*4)*56*1024);
  auto rw=mkrsrc(ws+OFF_WDT+(size_t)(eb*7+(q&1)*4)*1024);
  int coff[4];
  #pragma unroll
  for(int j=0;j<4;j++){ const int r=8*j+(lane>>3); const int swz=((lane>>4)&1)|(j<<1); coff[j]=r*4096+(((lane&7)^swz)<<4); }
  const int boff=lane*16;
  const int foff=((tb&7)*2+eb*3)%14;
  auto issueYC=[&](int s,int slot){
    LAS char* d=shm+slot*Y_SLOT;
    #pragma unroll
    for(int j=0;j<4;j++) BLDS(rc,d+(wid*4+j)*1024,coff[j],s*128); };
  auto issueYB=[&](int s,int slot){
    LAS char* d=shm+slot*Y_SLOT;
    #pragma unroll
    for(int j=0;j<CNT-4;j++){ const int p=wid+4*j; const int kk=p>=7, et=p-7*kk; BLDS_SC1(rb_,d+16384+p*1024,boff,((s*2+kk)*28+et)*1024); } };
  auto issueY=[&](int s,int slot){ issueYC(s,slot); issueYB(s,slot); };
  const int w3off=(q&1)?0x7ffffff0:boff;
  auto issueF=[&](int k){
    LAS char* d=shm+((k+1)&3)*F_SLOT; int kr=k+foff; kr=(kr>=14)?kr-14:kr; const int blk=4*kr+wid;
    #pragma unroll
    for(int j=0;j<4;j++) BLDS_SC1(rh_,d+(wid*4+j)*1024,boff,(j*56+blk)*1024);
    #pragma unroll
    for(int e=0;e<4;e++) BLDS_SC1(rw,d+16384+(wid*4+e)*1024,(e<3)?boff:w3off,(blk*28+e)*1024);
  };
  issueYC(0,0); issueYC(1,1); issueYC(2,2);
  {
    const int c0=(eb*112)>>6, c1=(eb*112+111)>>6, rb=(q&1)*8+(lane&7), wv=(lane>>3)&3;
    const int ca=c0+(lane>>5), cb=c1;
    const char* fa=ws+OFF_S1F+(size_t)(((rb*14+(q>>1)*7+ca)*4)+wv)*8; const char* fb=ws+OFF_S1F+(size_t)(((rb*14+(q>>1)*7+cb)*4)+wv)*8;
    LAS volatile unsigned* go=(LAS volatile unsigned*)(shm+PF_DUMMY);
    unsigned spins=0;
    if(wid==0){
      for(;;){
        const u64 va=__hip_atomic_load((GAS u64*)fa,RLX_AGENT), vb=__hip_atomic_load((GAS u64*)fb,RLX_AGENT);
        if(__all(va==tag && vb==tag) || ++spins>SPIN_CAP) break;
        __builtin_amdgcn_s_sleep(1);
      }
      *go=1u;
    } else {
      while(__builtin_amdgcn_readfirstlane(*go)==0u && ++spins<(SPIN_CAP<<4)) __builtin_amdgcn_s_sleep(2);
    }
    asm volatile("":::"memory");
  }
  issueYB(0,0); issueYB(1,1); issueYB(2,2);
  waitv<2*(CNT-4)>(); BAR();
  waitv<CNT-4>(); BAR();
  issueY(3,3); issueY(4,4);
  {
    auto rX=__builtin_amdgcn_make_buffer_rsrc((void*)x,0,2048*896,0x00020000);
    #pragma unroll
    for(int j=0;j<2;j++){ const int r=(wid*2+j)*8+(lane>>3); BLDS(rX,shm+XQ_BASE+(wid*2+j)*1024,(tb*128+(q>>1)*64+r)*896+eb*224+(q&1)*128+(lane&7)*16,0); }
  }
  int islot=0;
  #pragma unroll 1
  for(int s=1;s<9;s++){
    waitv<2*CNT>(); BAR();     issueY(s+4,islot); islot=(islot==4)?0:islot+1;
  }
  const char* pp=(lane<14)?(ws+OFF_S2F+(size_t)(tb*14+lane)*8):(ws+OFF_HFW+(size_t)(lane&31)*128);
  waitv<2*CNT>(); BAR();   u64 pv=tag; if(wid==0) pv=ld64_sc1(pp);
  issueY(13,3);
  waitv<2*CNT>(); BAR();   issueY(14,4);
  waitv<2*CNT>(); BAR();   issueY(15,0);
  waitv<2*CNT>(); if(wid==0){ pin64<2*CNT>(pv); spin64(pv,pp,tag); } BAR();
  waitv<CNT>(); BAR();
  issueF(0);
  waitv<8>(); BAR();
  issueF(1);
  BAR();
  issueF(2);
  const int psrc=(lane>>1)%3; const char* pq=ws+OFF_PF+(size_t)(((tile*4+q)*4+psrc+(psrc>=q?1:0))*2+(lane&1))*32;
  u64 pw=0;
  static_for<0,14>([&](auto kc){ constexpr int k=kc.v;
    constexpr int last=(k+2<=13)?k+2:13;
    waitv<8*(last-k)>();
    if constexpr(k==PS_CHECK){ if(wid==0){ pin64<8*((PS_CHECK+2<=13?PS_CHECK+2:13)-(PS_SAMPLE+2))>(pw); spin64(pw,pq,tag); } }
    BAR();
    if constexpr(k==PS_SAMPLE){ if(wid==0) pw=ld64_sc1(pq); }
    if constexpr(k+3<=13) issueF(k+3);
  });
  BAR();
  BAR();
}

__global__ __launch_bounds__(512,2) void fnet_geglu(const u16* __restrict__ x,const u16* __restrict__ Cr,const u16* __restrict__ Ci,const u16* __restrict__ Wr,const u16* __restrict__ Wi,
                                                    const u16* __restrict__ Wg,const u16* __restrict__ Wu,const u16* __restrict__ Wd,char* __restrict__ ws,u16* __restrict__ out){
  extern __shared__ __attribute__((aligned(16))) char smem[];
  LAS char* shm=(LAS char*)smem;
  const int tid=threadIdx.x, wid8=__builtin_amdgcn_readfirstlane(tid>>6), lane=tid&63, wid=wid8&3;
  const int bid=blockIdx.x, xcd=bid&7, idx=bid>>3;
  const bool gemm=idx<28;
  const int rbk=(xcd>>1)*4+idx/7, cg=(xcd&1)*7+idx%7, jb=rbk*14+cg;
  const int q=xcd>>1, tb=(xcd&1)*8+(idx>>2), eb=idx&3, tile=tb*4+eb;
  u64 tag=0;
  if(tid==0) *(LAS volatile unsigned*)(shm+PF_DUMMY)=0u;
  if(!gemm && wid8==7){
    auto rpc=__builtin_amdgcn_make_buffer_rsrc((void*)(__builtin_amdgcn_s_getpc()&~127ull),0,CODE_PF_BYTES,0x00020000);
    #pragma unroll
    for(int k=0;k<(CODE_PF_BYTES/128+63)/64;k++) __builtin_amdgcn_raw_ptr_buffer_load_lds(rpc,(LAS void*)(shm+PF_DUMMY+256),4,(k*64+lane)*128,0,0,0);
  }
  if(!gemm && wid8==6){
    auto rws=__builtin_amdgcn_make_buffer_rsrc((void*)ws,0,26u<<20,0x00020000);
    __builtin_amdgcn_raw_ptr_buffer_load_lds(rws,(LAS void*)(shm+PF_DUMMY+1024),4,lane*(512<<10),0,0,0);
    auto rou=__builtin_amdgcn_make_buffer_rsrc((void*)out,0,2048*448*2,0x00020000);
    __builtin_amdgcn_raw_ptr_buffer_load_lds(rou,(LAS void*)(shm+PF_DUMMY+1024),4,lane*(32<<10),0,0,0);
  }
  if(!gemm) helper(x,Wr,Wi,Wg,Wu,Wd,ws,shm,xcd*4+(idx-28),tag);
  if(wid8>=4){
    auto rcp=__builtin_amdgcn_make_buffer_rsrc((void*)((const char*)(q<2?Cr:Ci)+(size_t)(tb*128+eb*32+wid*8)*4096+(q&1)*2048),0,8*4096,0x00020000);
    auto prefetchC=[&](int c0,int nc){ __builtin_amdgcn_raw_ptr_buffer_load_lds(rcp,(LAS void*)(shm+PF_DUMMY+512),4,(lane<8*nc)?((lane&7)*4096+(lane>>3)*128):32768,c0*128,0,0); };
    if(!gemm){ prefetchC(0,8); prefetchC(8,8); }
    if(gemm){
      u64 g0=ld64_sc1(ws+OFF_G);
      auto rx=mkrsrc((const char*)x+(size_t)rbk*128*896);
      auto rd=mkrsrc((const char*)(cg<7?Wr:Wi)+(size_t)(cg%7)*64*896);
      auto rg=mkrsrc((const char*)Wg+cg*256);
      auto ru=mkrsrc((const char*)Wu+cg*256);
      int xo[4];
      #pragma unroll
      for(int j=0;j<4;j++){ const int r=wid*32+8*j+(lane>>3); const int swz=((lane>>4)&1)|(j<<1); xo[j]=r*896+(((lane&7)^swz)<<4); }
      int dofs[2];
      #pragma unroll
      for(int j=0;j<2;j++){ const int r=wid*16+8*j+(lane>>3); dofs[j]=r*896+(((lane&7)^((r>>1)&7))<<4); }
      int go[2];
      #pragma unroll
      for(int j=0;j<2;j++){ const int r=(wid*2+j)*4+(lane>>4); const int f=(r&3)|(((r>>3)&1)<<2); go[j]=r*3584+(((lane&15)^(f<<1))<<4); }
      auto issueXD=[&](int s){ LAS char* d=shm+(s%3)*XD_SLOT;
        #pragma unroll
        for(int j=0;j<4;j++) BLDS(rx,d+(wid*4+j)*1024,xo[j],s*128);
        #pragma unroll
        for(int j=0;j<2;j++) BLDS(rd,d+16384+(wid*2+j)*1024,dofs[j],s*128); };
      auto issueGU=[&](int j32){ LAS char* d=shm+GU_BASE+(j32%5)*GU_SLOT;
        #pragma unroll
        for(int j=0;j<2;j++) BLDS(rg,d+(wid*2+j)*1024,go[j],j32*32*3584);
        #pragma unroll
        for(int j=0;j<2;j++) BLDS(ru,d+8192+(wid*2+j)*1024,go[j],j32*32*3584); };
      auto rs=__builtin_amdgcn_make_buffer_rsrc((void*)ws,0,262144,0x00020000);
      issueXD(0); issueGU(0); issueGU(1); issueXD(1); issueGU(2);
      pin64<24>(g0); tag=bcast64(g0)+1;
      waitv<14>(); BAR();
      u32x4 tv0,tv1,tv2; u64 pv=tag; const char* pa=ws+OFF_HFA+(size_t)(lane&31)*128;
      static_for<0,14>([&](auto jc){ constexpr int j=jc.v;
        waitv<k1_issued(j)-k1_need(j)+((j>=11)?3+(j-10):0)>();
        if constexpr(j==10){ if(wid==0){ pin64<14>(pv); spin64(pv,pa,tag); } }
        BAR();
        if constexpr(j==0){ issueGU(3); issueXD(2); issueGU(4); }
        if constexpr(j==8){ if(wid==0) pv=ld64_sc1(pa); }
        if constexpr(j>=2 && (j&1)==0 && j/2+2<=6) issueXD(j/2+2);
        if constexpr(j>=1 && j+4<=13) issueGU(j+4);
        if constexpr(j==10){
          const int gsrc=(int)OFF_CSP+cg*4096, usrc=(int)OFF_CSP+(14+cg)*4096;
          int o0,o1,o2;
          if(wid==0){ o0=(int)OFF_STATS+rbk*1024+lane*16; o1=gsrc+lane*16; o2=gsrc+1024+lane*16; }
          else if(wid==1){ o0=(int)OFF_CSD+cg*256+(lane&15)*16; o1=gsrc+2048+lane*16; o2=gsrc+3072+lane*16; }
          else { o0=usrc+(wid-2)*2048+lane*16; o1=o0+1024; o2=o0; }
          asm volatile("buffer_load_dwordx4 %0, %3, %6, 0 offen sc1\n\tbuffer_load_dwordx4 %1, %4, %6, 0 offen sc1\n\tbuffer_load_dwordx4 %2, %5, %6, 0 offen sc1"
                       :"=&v"(tv0),"=&v"(tv1),"=&v"(tv2):"v"(o0),"v"(o1),"v"(o2),"s"(rs):"memory");
        }
        if constexpr(j==12){
          asm volatile("s_waitcnt vmcnt(2)":"+v"(tv0),"+v"(tv1),"+v"(tv2)::"memory");
          LAS char* tb_=shm+TB_BASE+lane*16;
          if(wid==0){ *(LAS u32x4*)(tb_)=tv0; *(LAS u32x4*)(tb_+2048)=tv1; *(LAS u32x4*)(tb_+3072)=tv2; }
          else if(wid==1){ *(LAS u32x4*)(tb_+1024)=tv0; *(LAS u32x4*)(tb_+4096)=tv1; *(LAS u32x4*)(tb_+5120)=tv2; }
          else { *(LAS u32x4*)(tb_+6144+(wid-2)*2048)=tv0; *(LAS u32x4*)(tb_+7168+(wid-2)*2048)=tv1; }
          LGKM0();
        }
        if constexpr(j>=10) prefetchC(PFN*(j-10),PFN);
        if constexpr(j==13){
          if(wid==0){
            LAS char* src=shm+TB_BASE+2048+(lane>>5)*4096+(lane&31)*16;
            f32x4 s={0,0,0,0};
            #pragma unroll
            for(int r=0;r<8;r++) s+=*(LAS f32x4*)(src+r*512);
            *(LAS f32x4*)(shm+TB_BASE+10240+lane*16)=s;
            LGKM0();
          }
        }
      });
      BAR();
      BAR();
    }
    asm volatile("":::"memory");
    if(wid<2) loader2<8>(x,Cr,Ci,ws,shm,wid,lane,q,tb,eb,tag); else loader2<7>(x,Cr,Ci,ws,shm,wid,lane,q,tb,eb,tag);
    return;
  }
  __builtin_amdgcn_s_setprio(1);
  const int g=lane>>4, i=lane&15;
  if(gemm){
    tag=bcast64(__hip_atomic_load((GAS u64*)(ws+OFF_G),RLX_AGENT))+1;
    const int wt=wid>>1, wf=wid&1;
    const int xsw=((i>>1)&1)|((i>>2)<<1);
    const unsigned xa0=(wt*64+8*(i>>2)+(i&3))*128+((g^xsw)<<4), xa1=xa0^64;
    const unsigned da0=16384+(wf*32+i)*128+((g^((i>>1)&7))<<4), da1=da0^64;
    const int q4=(lane&15)>>2, p4=lane&3, fsw=q4|((g&1)<<2);
    unsigned wa[4];
    #pragma unroll
    for(int j=0;j<4;j++) wa[j]=GU_BASE+(8*g+q4)*256+(((4*wf+j)^fsw)<<5)+8*p4;
    f32x4 ag[4][4], au[4][4], aa[4][2];
    #pragma unroll
    for(int u=0;u<4;u++){
      #pragma unroll
      for(int j=0;j<4;j++){ ag[j][u]=(f32x4){0,0,0,0}; au[j][u]=(f32x4){0,0,0,0}; asm volatile("":"+v"(ag[j][u]),"+v"(au[j][u])); }
      aa[u][0]=(f32x4){0,0,0,0}; aa[u][1]=(f32x4){0,0,0,0}; asm volatile("":"+v"(aa[u][0]),"+v"(aa[u][1])); }
    auto rdW=[&](LAS char* d,unsigned a)->bf16x8{
      s16x4 lo=__builtin_amdgcn_ds_read_tr16_b64_v4i16((LAS s16x4*)(d+a)), hi=__builtin_amdgcn_ds_read_tr16_b64_v4i16((LAS s16x4*)(d+a+1024));
      return __builtin_shufflevector(lo,hi,0,1,2,3,4,5,6,7); };
    BAR();
    if(tid==0) __hip_atomic_store((GAS u64*)(ws+OFF_SEEN)+bid,tag,RLX_AGENT);
    bf16x8 xf[4], w0, w1;
    #pragma unroll
    for(int u=0;u<4;u++) xf[u]=*(LAS bf16x8*)(shm+xa0+(u>>1)*4096+(u&1)*512);
    w0=rdW(shm,wa[0]); w1=rdW(shm,wa[0]+8192);
    LGKM0();
    int xs=0, gs=0;
    #pragma unroll 1
    for(int s=0;s<7;s++){
      LAS char* dx=shm+xs*XD_SLOT; const int xn_=(xs==2)?0:xs+1; LAS char* dxn=shm+xn_*XD_SLOT;
      #pragma unroll
      for(int kk=0;kk<2;kk++){
        BAR();
        LAS char* dw=shm+gs*GU_SLOT; const int gn=(gs==4)?0:gs+1; LAS char* nw=shm+gn*GU_SLOT;
        LAS char* nx=(kk==0)?dx:dxn; const unsigned nxa=(kk==0)?xa1:xa0;
        bf16x8 xn[4], df[2];
        bf16x8 wA=w0, wB=w1;
        #pragma unroll
        for(int f=0;f<8;f++){
          const int fn=f+2;
          bf16x8 wC;
          if(fn<8) wC=rdW(dw,wa[fn>>1]+(fn&1)*8192); else wC=rdW(nw,wa[0]+(fn&1)*8192);
          if(f<2) df[f]=*(LAS bf16x8*)(dx+(kk?da1:da0)+f*2048);
          if(f>=4) xn[f-4]=*(LAS bf16x8*)(nx+nxa+((f-4)>>1)*4096+((f-4)&1)*512);
          const int j=f>>1;
          #pragma unroll
          for(int u=0;u<4;u++){ if(f&1) au[j][u]=MFMA16(wA,xf[u],au[j][u]); else ag[j][u]=MFMA16(wA,xf[u],ag[j][u]); }
          wA=wB; wB=wC;
        }
        #pragma unroll
        for(int u=0;u<4;u++){ aa[u][0]=MFMA16(xf[u],df[0],aa[u][0]); aa[u][1]=MFMA16(xf[u],df[1],aa[u][1]); }
        w0=wA; w1=wB;
        #pragma unroll
        for(int u=0;u<4;u++) xf[u]=xn[u];
        #pragma unroll
        for(int f=0;f<2;f++){ SGB(0x8,1); SGB(0x100,1); SGB(0x8,1); SGB(0x100,1); SGB(0x8,1); SGB(0x100,1); SGB(0x8,1); }
        #pragma unroll
        for(int f=0;f<2;f++){ SGB(0x8,1); SGB(0x100,1); SGB(0x8,1); SGB(0x100,1); SGB(0x8,2); }
        #pragma unroll
        for(int f=0;f<4;f++){ SGB(0x8,1); SGB(0x100,1); SGB(0x8,1); SGB(0x100,1); SGB(0x8,1); SGB(0x100,1); SGB(0x8,1); }
        SGB(0x8,8);
        SB0(); LGKM0();
        gs=gn;
      }
      xs=xn_;
    }
    {
      int tz=threadIdx.x; asm volatile("":"+v"(tz));
      const int lane=tz&63, g=lane>>4, i=lane&15;
      LAS const float* lst=(LAS const float*)(shm+TB_BASE);
      LAS const float* lcd=(LAS const float*)(shm+TB_BASE+1024);
      LAS const float* lcg=(LAS const float*)(shm+TB_BASE+10240);
      const float sgn=(cg<7)?1.f:-1.f;
      const float c0=lcd[wf*32+i]*sgn, c1=lcd[wf*32+16+i]*sgn;
      auto rab=__builtin_amdgcn_make_buffer_rsrc((void*)(ws+OFF_AB+(size_t)(cg<7?0:64*28)*1024),0,64*28*1024,0x00020000);
      #pragma unroll
      for(int G=0;G<2;G++){
        unsigned w0_[4], w1_[4];
        #pragma unroll
        for(int p=0;p<4;p++){ const int n=p>>1, r=(p&1)*2;
          const f32x4 s4=*(LAS const f32x4*)(lst+(wt*64+G*32+8*g+2*p)*2);
          const float ra=s4[1]*sgn, rb_=s4[3]*sgn, ma=s4[0]*s4[1], mb=s4[2]*s4[3];
          w0_[p]=pk2(aa[2*G+n][0][r]*ra-ma*c0, aa[2*G+n][0][r+1]*rb_-mb*c0);
          w1_[p]=pk2(aa[2*G+n][1][r]*ra-ma*c1, aa[2*G+n][1][r+1]*rb_-mb*c1);
        }
        const int sblk=rbk*4+wt*2+G, et=(cg%7)*4+wf*2;
        BST16(((u32x4){w0_[0],w0_[1],w0_[2],w0_[3]}),rab,(lane*16)+((sblk*28+et)*1024),16);
        BST16(((u32x4){w1_[0],w1_[1],w1_[2],w1_[3]}),rab,(lane*16)+((sblk*28+et+1)*1024),16);
      }
      BAR();
      f32x2 st[4]; f32x4 cgv[4],cuv[4];
      #pragma unroll
      for(int u=0;u<4;u++){ const int t=wt*64+(u>>1)*32+8*(i>>2)+4*(u&1)+(i&3); st[u]=*(LAS const f32x2*)(lst+t*2); }
      #pragma unroll
      for(int j=0;j<4;j++){ const int Fc=wf*64+j*16+4*g; cgv[j]=*(LAS const f32x4*)(lcg+Fc); cuv[j]=*(LAS const f32x4*)(lcg+128+Fc); }
      asm volatile("s_waitcnt lgkmcnt(0)":"+v"(st[0]),"+v"(st[1]),"+v"(st[2]),"+v"(st[3]),"+v"(cgv[0]),"+v"(cgv[1]),"+v"(cgv[2]),"+v"(cgv[3]),"+v"(cuv[0]),"+v"(cuv[1]),"+v"(cuv[2]),"+v"(cuv[3])::"memory");
      BAR();
      auto rhh=__builtin_amdgcn_make_buffer_rsrc((void*)(ws+OFF_H),0,128*56*1024,0x00020000);
      #pragma unroll
      for(int jp=0;jp<2;jp++){
        const int blk=cg*4+wf*2+jp;
        #pragma unroll
        for(int u=0;u<4;u++){
          unsigned w[4];
          const f32x2 m2={st[u][0],st[u][0]}, r2={st[u][1],st[u][1]};
          #pragma unroll
          for(int jj=0;jj<2;jj++)
          #pragma unroll
          for(int p=0;p<2;p++){
            const f32x4 ag4=ag[2*jp+jj][u], au4=au[2*jp+jj][u], c4=cgv[2*jp+jj], u4=cuv[2*jp+jj];
            const f32x2 a2={ag4[2*p],ag4[2*p+1]}, u2={au4[2*p],au4[2*p+1]}, cg2={c4[2*p],c4[2*p+1]}, cu2={u4[2*p],u4[2*p+1]};
            w[2*jj+p]=geglu2(a2,u2,cg2,cu2,m2,r2);
          }
          const int tf=rbk*8+wt*4+u;
          BST16(((u32x4){w[0],w[1],w[2],w[3]}),rhh,(lane*16)+((tf*56+blk)*1024),16);
          if(jp==0 && u==AB_U){
            waitv<AB_U+1>();
            if(lane==0) __hip_atomic_store((GAS u64*)(ws+OFF_S1F)+jb*4+wid,tag,RLX_AGENT);
          }
        }
      }
    }
  }
  {
    int tz=threadIdx.x; asm volatile("":"+v"(tz));
    const int lane=tz&63, g=lane>>4, i=lane&15;
    const int swz=((i>>1)&1)|((i>>2)<<1);
    const unsigned ca0=(wid*32+8*(i>>2)+(i&3))*128+((g^swz)<<4), ca1=ca0^64;
    const unsigned ba=16384+lane*16;
    f32x4 acc[7][2];
    #pragma unroll
    for(int e=0;e<7;e++){ acc[e][0]=(f32x4){0,0,0,0}; acc[e][1]=(f32x4){0,0,0,0}; asm volatile("":"+v"(acc[e][0]),"+v"(acc[e][1])); }
    BAR();
    bf16x8 c0,c1,b[7];
    c0=*(LAS bf16x8*)(shm+ca0); c1=*(LAS bf16x8*)(shm+ca0+512);
    #pragma unroll
    for(int e=0;e<7;e++) b[e]=*(LAS bf16x8*)(shm+ba+e*1024);
    LGKM0();
    int slot=0;
    auto stepy=[&](auto mc){ constexpr int MODE=mc.v;
      BAR();
      if constexpr(MODE==2){ if(gemm && tid==0) __hip_atomic_store((GAS u64*)(ws+OFF_S2F)+jb,tag,RLX_AGENT); }
      LAS char* d=shm+slot*Y_SLOT; const int ns=(slot==4)?0:slot+1; LAS char* dn=shm+ns*Y_SLOT;
      bf16x8 e0=*(LAS bf16x8*)(d+ca1), e1=*(LAS bf16x8*)(d+ca1+512), f[7];
      #pragma unroll
      for(int e=0;e<7;e++) f[e]=*(LAS bf16x8*)(d+ba+(7+e)*1024);
      #pragma unroll
      for(int e=0;e<7;e++){ acc[e][0]=MFMA16(b[e],c0,acc[e][0]); acc[e][1]=MFMA16(b[e],c1,acc[e][1]); }
      c0=*(LAS bf16x8*)(dn+ca0); c1=*(LAS bf16x8*)(dn+ca0+512);
      #pragma unroll
      for(int e=0;e<7;e++) b[e]=*(LAS bf16x8*)(dn+ba+e*1024);
      #pragma unroll
      for(int e=0;e<7;e++){ acc[e][0]=MFMA16(f[e],e0,acc[e][0]); acc[e][1]=MFMA16(f[e],e1,acc[e][1]); }
      #pragma unroll
      for(int h=0;h<2;h++){
        #pragma unroll
        for(int u=0;u<9;u++){ SGB(0x8,1); SGB(0x100,1); }
        SGB(0x8,5); }
      SB0(); LGKM0();
      if constexpr(MODE==1) waitv<0>();
      slot=ns;
    };
    stepy(IC<0>{}); stepy(IC<1>{}); stepy(IC<2>{});
    #pragma unroll 1
    for(int s=3;s<16;s++) stepy(IC<0>{});
    u32x4 pk[7];
    #pragma unroll
    for(int n=0;n<2;n++)
    #pragma unroll
    for(int p=0;p<3;p++) pk[3*n+p]=(u32x4){pk2(acc[2*p][n][0],acc[2*p][n][1]),pk2(acc[2*p][n][2],acc[2*p][n][3]),pk2(acc[2*p+1][n][0],acc[2*p+1][n][1]),pk2(acc[2*p+1][n][2],acc[2*p+1][n][3])};
    pk[6]=(u32x4){pk2(acc[6][0][0],acc[6][0][1]),pk2(acc[6][0][2],acc[6][0][3]),pk2(acc[6][1][0],acc[6][1][1]),pk2(acc[6][1][2],acc[6][1][3])};
    const int hh=q>>1, eg=q&1;
    const int d0=(wid>>1)*2, d1=d0+1;
    auto rp=__builtin_amdgcn_make_buffer_rsrc((void*)(ws+OFF_PART+(size_t)(tile*32)*PREC),0,32*PREC,0x00020000);
    const int o0=((d0*4+q)*2+(wid&1))*PREC+lane*16, o1=((d1*4+q)*2+(wid&1))*PREC+lane*16;
    const int kb=wid&1, th=wid>>1, n_=wid&1;
    auto fpart=[&](auto nec){ constexpr int NE=nec.v;
      f32x4 af[NE][2];
      #pragma unroll
      for(int e=0;e<NE;e++){ af[e][0]=(f32x4){0,0,0,0}; af[e][1]=(f32x4){0,0,0,0}; }
      u32x4 pin_[3][2];
      const int sa=(NE==4)?(2*n_):n_, sb=(NE==4)?(2*n_+1):2;
      static_for<0,14>([&](auto kc){ constexpr int k=kc.v;
        BAR();
        LAS char* d=shm+((k+1)&3)*F_SLOT;
        bf16x8 hf[2][2], w[2][NE];
        #pragma unroll
        for(int h=0;h<2;h++){
          const int bl=kb+2*h;
          hf[h][0]=*(LAS bf16x8*)(d+(bl*4+th*2+0)*1024+lane*16); hf[h][1]=*(LAS bf16x8*)(d+(bl*4+th*2+1)*1024+lane*16);
          #pragma unroll
          for(int e=0;e<NE;e++) w[h][e]=*(LAS bf16x8*)(d+16384+(bl*4+e)*1024+lane*16);
        }
        if constexpr(k==PS_FLAG){ waitv<0>();
          if(lane==0){
            if(d0!=q) __hip_atomic_store((GAS u64*)(ws+OFF_PF+(size_t)(((tile*4+d0)*4+q)*2+(wid&1))*32),tag,RLX_AGENT);
            if(d1!=q) __hip_atomic_store((GAS u64*)(ws+OFF_PF+(size_t)(((tile*4+d1)*4+q)*2+(wid&1))*32),tag,RLX_AGENT); }
 }
        if constexpr(k==PS_CHECK){
          #pragma unroll
          for(int kk=0;kk<3;kk++){ const int src=kk+(kk>=q?1:0); const int o=((q*4+src)*2+th)*PREC+lane*16;
            pin_[kk][0]=__builtin_amdgcn_raw_buffer_load_b128(rp,o+sa*1024,0,16);
            pin_[kk][1]=__builtin_amdgcn_raw_buffer_load_b128(rp,o+sb*1024,0,16); }
        }
        #pragma unroll
        for(int h=0;h<2;h++)
        #pragma unroll
        for(int e=0;e<NE;e++){ af[e][0]=MFMA16(w[h][e],hf[h][0],af[e][0]); af[e][1]=MFMA16(w[h][e],hf[h][1],af[e][1]); }
        SGB(0x100,2+NE);
        #pragma unroll
        for(int u=0;u<2+NE;u++){ SGB(0x100,1); SGB(0x8,1); }
        SGB(0x8,4*NE-2-NE);
        SB0();
        if constexpr(k==0){ if(d0!=q){ BST16(pk[0],rp,o0,16); BST16(pk[1],rp,o0+1024,16); BST16(pk[3],rp,o0+2048,16); BST16(pk[4],rp,o0+3072,16); } }
        if constexpr(k==PS_ST2){ if(d1!=q){ BST16(pk[2],rp,o1,16); BST16(pk[5],rp,o1+1024,16); BST16(pk[6],rp,o1+2048,16); } }
      });
      BAR();
      if((wid>>1)==hh){
        LAS char* o=shm+(wid&1)*4096+lane*16;
        if(NE==4){ *(LAS u32x4*)(o)=pk[0]; *(LAS u32x4*)(o+1024)=pk[1]; *(LAS u32x4*)(o+2048)=pk[3]; *(LAS u32x4*)(o+3072)=pk[4]; }
        else { *(LAS u32x4*)(o)=pk[2]; *(LAS u32x4*)(o+1024)=pk[5]; *(LAS u32x4*)(o+2048)=pk[6]; }
      }
      #pragma unroll
      for(int e=0;e<NE;e++) *(LAS f32x4*)(shm+8192+wid*4096+e*1024+lane*16)=n_?af[e][0]:af[e][1];
      LGKM0();
      BAR();
      const int trl=th*32+8*(i>>2)+4*n_+(i&3), trow=tb*128+hh*64+trl;
      u32x4 yo[2];
      yo[0]=*(LAS u32x4*)(shm+th*4096+sa*1024+lane*16); yo[1]=*(LAS u32x4*)(shm+th*4096+sb*1024+lane*16);
      #pragma unroll
      for(int e=0;e<NE;e++){
        const f32x4 fo=*(LAS f32x4*)(shm+8192+(wid^1)*4096+e*1024+lane*16);
        const f32x4 fm=n_?af[e][1]:af[e][0];
        const f32x4 f4=kb?(fo+fm):(fm+fo);
        const u32x2 xv=*(LAS u32x2*)(shm+XQ_BASE+trl*128+e*32+8*g);
        unsigned res[2];
        #pragma unroll
        for(int p=0;p<2;p++){
          auto sel=[&](const u32x4& a,const u32x4& b)->unsigned{ if(e<2) return a[(e&1)*2+p]; if(NE==4) return b[(e&1)*2+p]; return n_?b[2+p]:b[p]; };
          const unsigned y0=sel(yo[0],yo[1]), y1=sel(pin_[0][0],pin_[0][1]), y2=sel(pin_[1][0],pin_[1][1]), y3=sel(pin_[2][0],pin_[2][1]);
          const f32x2 ff={f4[2*p],f4[2*p+1]};
          const f32x2 z=ff*(-1.4426950408889634f);
          f32x2 ex; ex.x=__builtin_amdgcn_exp2f(z.x); ex.y=__builtin_amdgcn_exp2f(z.y);
          const f32x2 dn=ex+1.0f;
          f32x2 rc; rc.x=__builtin_amdgcn_rcpf(dn.x); rc.y=__builtin_amdgcn_rcpf(dn.y);
          const f32x2 y=((f32x2){bflo(y0),bfhi(y0)}+(f32x2){bflo(y1),bfhi(y1)})+((f32x2){bflo(y2),bfhi(y2)}+(f32x2){bflo(y3),bfhi(y3)});
          const f32x2 r=y*(ff*rc)+(f32x2){bflo(xv[p]),bfhi(xv[p])};
          res[p]=pk2(r.x,r.y);
        }
        *(u32x2*)(out+(size_t)trow*D_+eb*112+(4*eg+e)*16+4*g)=(u32x2){res[0],res[1]};
      }
    };
    if(eg==0) fpart(IC<4>{}); else fpart(IC<3>{});
  }
}
}

extern "C" void kernel_launch(void* const* d_in, const int* in_sizes, int n_in, void* d_out, int out_size, void* d_ws, size_t ws_size, hipStream_t stream){
  const u16* x=(const u16*)d_in[0]; const u16* Cr=(const u16*)d_in[1]; const u16* Ci=(const u16*)d_in[2];
  const u16* Wr=(const u16*)d_in[3]; const u16* Wi=(const u16*)d_in[4];
  const u16* Wg=(const u16*)d_in[6]; const u16* Wu=(const u16*)d_in[7]; const u16* Wd=(const u16*)d_in[8];
  (void)hipFuncSetAttribute((const void*)fnet_geglu,hipFuncAttributeMaxDynamicSharedMemorySize,163840);
  fnet_geglu<<<256,512,163840,stream>>>(x,Cr,Ci,Wr,Wi,Wg,Wu,Wd,(char*)d_ws,(u16*)d_out);
}
```
